```python
import torch

def _softmax(x, axis=-1):
    return torch.softmax(x, dim=axis)
def _gelu(x):
    return torch.nn.functional.gelu(x, approximate="tanh")
def _silu(x):
    return torch.nn.functional.silu(x)
def _sigmoid(x):
    return torch.sigmoid(x)
def _layernorm(x, axis=-1, eps=1e-5):
    m = x.mean(dim=axis, keepdim=True)
    v = x.var(dim=axis, keepdim=True, unbiased=False)
    return (x - m) / torch.sqrt(v + eps)
def _scatter_add(tgt, idx, src, axis=0):
    return tgt.clone().index_add_(axis, idx, src)
def _segment_sum(src, seg, n):
    out = torch.zeros((n,) + src.shape[1:], dtype=src.dtype)
    return out.index_add_(0, seg, src)
def _pad(x, pw):
    flat = tuple(v for pair in reversed(pw) for v in pair)
    return torch.nn.functional.pad(x, flat)

kernel_name = 'concat_heads__compressed_kv_cla__compressed_kv_cla__decode'

def setup_inputs():

    _w = (lambda a: (lambda t: t.to(torch.bfloat16) if t.is_floating_point() else t)(torch.as_tensor(a)))
    def _A_setup():
        B, H, Sq, Sk, D, M = 4, 32, 1, 4096, 128, 512
        Dc = 4 * D
        return {
            "x": _w((lambda *s: torch.randn(*s, dtype=torch.bfloat16) * 3.2)(B, Sq, M)),
            "c_kv": _w((lambda *s: torch.randn(*s, dtype=torch.bfloat16) * 0.05)(B, Sk, Dc)),
            "w_q": _w((lambda *s: torch.randn(*s, dtype=torch.bfloat16) * 3.2)(M, H, D) * (M ** -0.5)),
            "w_uk": _w((lambda *s: torch.randn(*s, dtype=torch.bfloat16) * 3.2)(Dc, H, D) * (Dc ** -0.5)),
            "w_uv": _w((lambda *s: torch.randn(*s, dtype=torch.bfloat16) * 0.05)(Dc, H, D) * (Dc ** -0.5)),
            "w_o": _w((lambda *s: torch.randn(*s, dtype=torch.bfloat16) * 0.05)(H, D, M) * ((H * D) ** -0.5)),
        }
    _A_in = _A_setup()

    def _B_setup():
        B, H, Sq, Sk, D, M = 4, 32, 1, 4096, 128, 512
        Dc = 4 * D
        return {
            "x": _w((lambda *s: torch.randn(*s, dtype=torch.bfloat16) * 3.2)(B, Sq, M)),
            "c_kv": _w((lambda *s: torch.randn(*s, dtype=torch.bfloat16) * 0.05)(B, Sk, Dc)),
            "w_q": _w((lambda *s: torch.randn(*s, dtype=torch.bfloat16) * 3.2)(M, H, D) * (M ** -0.5)),
            "w_uk": _w((lambda *s: torch.randn(*s, dtype=torch.bfloat16) * 3.2)(Dc, H, D) * (Dc ** -0.5)),
            "w_uv": _w((lambda *s: torch.randn(*s, dtype=torch.bfloat16) * 0.05)(Dc, H, D) * (Dc ** -0.5)),
            "w_o": _w((lambda *s: torch.randn(*s, dtype=torch.bfloat16) * 0.05)(H, D, M) * ((H * D) ** -0.5)),
        }
    _B_in = _B_setup()

    return {
        "A_x": _A_in["x"],
        "A_c_kv": _A_in["c_kv"],
        "A_w_q": _A_in["w_q"],
        "A_w_uk": _A_in["w_uk"],
        "A_w_uv": _A_in["w_uv"],
        "A_w_o": _A_in["w_o"],
        "B_x": _B_in["x"],
        "B_c_kv": _B_in["c_kv"],
        "B_w_q": _B_in["w_q"],
        "B_w_uk": _B_in["w_uk"],
        "B_w_uv": _B_in["w_uv"],
        "B_w_o": _B_in["w_o"],
    }

def reference(A_x, A_c_kv, A_w_q, A_w_uk, A_w_uv, A_w_o, B_x, B_c_kv, B_w_q, B_w_uk, B_w_uv, B_w_o):
    def _A_ref(x, c_kv, w_q, w_uk, w_uv, w_o):
        B, Sq, M = x.shape[0], x.shape[1], x.shape[2]
        Sk = c_kv.shape[1]
        H, D = w_q.shape[1], w_q.shape[2]
        q = torch.einsum("bqm,mhd->bhqd", x, w_q)
        k = torch.einsum("bkc,chd->bhkd", c_kv, w_uk)
        v = torch.einsum("bkc,chd->bhkd", c_kv, w_uv)
        scores = torch.einsum("bhqd,bhkd->bhqk", q, k) * (D ** -0.5)
        off = Sk - Sq
        qi = torch.arange(Sq)[:, None]
        kj = torch.arange(Sk)[None, :]
        cmask = kj <= (qi + off)
        scores = torch.where(cmask[None, None], scores, -1e9)
        p = _softmax(scores, axis=-1)
        ctx = torch.einsum("bhqk,bhkd->bhqd", p, v)
        return torch.einsum("bhqd,hdm->bqm", ctx, w_o)

    def _B_ref(x, c_kv, w_q, w_uk, w_uv, w_o):
        B, Sq, M = x.shape[0], x.shape[1], x.shape[2]
        Sk = c_kv.shape[1]
        H, D = w_q.shape[1], w_q.shape[2]
        q = torch.einsum("bqm,mhd->bhqd", x, w_q)
        k = torch.einsum("bkc,chd->bhkd", c_kv, w_uk)
        v = torch.einsum("bkc,chd->bhkd", c_kv, w_uv)
        scores = torch.einsum("bhqd,bhkd->bhqk", q, k) * (D ** -0.5)
        off = Sk - Sq
        qi = torch.arange(Sq)[:, None]
        kj = torch.arange(Sk)[None, :]
        cmask = kj <= (qi + off)
        scores = torch.where(cmask[None, None], scores, -1e9)
        p = _softmax(scores, axis=-1)
        ctx = torch.einsum("bhqk,bhkd->bhqd", p, v)
        return torch.einsum("bhqd,hdm->bqm", ctx, w_o)

    a = _A_ref(A_x, A_c_kv, A_w_q, A_w_uk, A_w_uv, A_w_o)
    b = _B_ref(B_x, B_c_kv, B_w_q, B_w_uk, B_w_uv, B_w_o)
    return torch.concatenate([a, b], axis=-1)
```

```cpp
#include <hip/hip_runtime.h>
#include <hip/hip_bf16.h>
#include <stdint.h>

typedef unsigned short bf16_t;
__device__ __forceinline__ float bf2f(bf16_t v) { return __uint_as_float(((unsigned)v) << 16); }
__device__ __forceinline__ bf16_t f2bf(float f) { unsigned u = __float_as_uint(f); u += 0x7FFFu + ((u >> 16) & 1u); return (bf16_t)(u >> 16); }

constexpr int B = 4, H = 32, SK = 4096, D = 128, M = 512, DC = 512, NCH = 32, CH = 128;

struct Ptrs { const bf16_t* x[2]; const bf16_t* ckv[2]; const bf16_t* wq[2]; const bf16_t* wuk[2]; const bf16_t* wuv[2]; const bf16_t* wo[2]; };

__global__ void k_q(Ptrs p, float* q) {
  int br = blockIdx.y, h = blockIdx.x, d = threadIdx.x;
  __shared__ float xs[B][M];
  for (int i = threadIdx.x; i < B * M; i += blockDim.x) xs[i / M][i % M] = bf2f(p.x[br][i]);
  __syncthreads();
  float a[B] = {0, 0, 0, 0};
  for (int m = 0; m < M; ++m) { float w = bf2f(p.wq[br][(size_t)m * H * D + h * D + d]);
    for (int b = 0; b < B; ++b) a[b] += xs[b][m] * w; }
  for (int b = 0; b < B; ++b) q[((br * B + b) * H + h) * D + d] = bf2f(f2bf(a[b]));
}
__global__ void k_qa(Ptrs p, const float* q, float* qa) {
  int br = blockIdx.z, h = blockIdx.y, c = blockIdx.x * 128 + threadIdx.x;
  __shared__ float qs[B][D];
  for (int i = threadIdx.x; i < B * D; i += blockDim.x) qs[i / D][i % D] = q[((br * B + i / D) * H + h) * D + i % D];
  __syncthreads();
  float a[B] = {0, 0, 0, 0};
  for (int d = 0; d < D; ++d) { float w = bf2f(p.wuk[br][(size_t)c * H * D + h * D + d]);
    for (int b = 0; b < B; ++b) a[b] += qs[b][d] * w; }
  for (int b = 0; b < B; ++b) qa[((br * B + b) * H + h) * DC + c] = a[b] * 0.08838834764831845f;
}
__global__ void k_attn(Ptrs p, const float* qa, float* op, float* ml) {
  int br = blockIdx.z, b = blockIdx.y, j = blockIdx.x, t = threadIdx.x;
  __shared__ float S[H][CH];
  __shared__ float qs[H][DC + 1];
  for (int i = t; i < H * DC; i += 256) qs[i / DC][i % DC] = qa[((br * B + b) * H) * DC + i];
  __syncthreads();
  const bf16_t* C = p.ckv[br] + ((size_t)b * SK + j * CH) * DC;
  for (int i = t; i < H * CH; i += 256) { int h = i % H, k = i / H; float a = 0;
    for (int c = 0; c < DC; ++c) a += qs[h][c] * bf2f(C[(size_t)k * DC + c]);
    S[h][k] = a; }
  __syncthreads();
  if (t < H) { float m = -INFINITY; for (int k = 0; k < CH; ++k) m = fmaxf(m, S[t][k]);
    float l = 0; for (int k = 0; k < CH; ++k) { float e = __expf(S[t][k] - m); S[t][k] = e; l += e; }
    ml[((((br * B + b) * NCH + j) * H) + t) * 2] = m; ml[((((br * B + b) * NCH + j) * H) + t) * 2 + 1] = l; }
  __syncthreads();
  for (int i = t; i < H * DC; i += 256) { int h = i / DC, c = i % DC; float a = 0;
    for (int k = 0; k < CH; ++k) a += S[h][k] * bf2f(C[(size_t)k * DC + c]);
    op[((((size_t)(br * B + b) * NCH + j) * H) + h) * DC + c] = a; }
}
__global__ void k_ctx(Ptrs p, const float* op, const float* ml, float* ctx) {
  int br = blockIdx.z, b = blockIdx.y, h = blockIdx.x, t = threadIdx.x;
  __shared__ float cc[DC]; __shared__ float w[NCH];
  if (t == 0) { float Mx = -INFINITY; for (int j = 0; j < NCH; ++j) Mx = fmaxf(Mx, ml[((((br * B + b) * NCH + j) * H) + h) * 2]);
    float L = 0; for (int j = 0; j < NCH; ++j) { float e = __expf(ml[((((br * B + b) * NCH + j) * H) + h) * 2] - Mx); w[j] = e; L += e * ml[((((br * B + b) * NCH + j) * H) + h) * 2 + 1]; }
    for (int j = 0; j < NCH; ++j) w[j] /= L; }
  __syncthreads();
  for (int c = t; c < DC; c += 128) { float a = 0; for (int j = 0; j < NCH; ++j) a += w[j] * op[((((size_t)(br * B + b) * NCH + j) * H) + h) * DC + c]; cc[c] = a; }
  __syncthreads();
  float a = 0; for (int c = 0; c < DC; ++c) a += cc[c] * bf2f(p.wuv[br][(size_t)c * H * D + h * D + t]);
  ctx[((br * B + b) * H + h) * D + t] = a;
}
__global__ void k_out(Ptrs p, const float* ctx, bf16_t* out) {
  int br = blockIdx.z, b = blockIdx.y, m = blockIdx.x * 64 + (threadIdx.x & 63), g = threadIdx.x >> 6;
  __shared__ float red[4][64];
  float a = 0; for (int k = g * 1024; k < g * 1024 + 1024; ++k) a += ctx[(br * B + b) * H * D + k] * bf2f(p.wo[br][(size_t)k * M + m]);
  red[g][threadIdx.x & 63] = a; __syncthreads();
  if (g == 0) out[b * 2 * M + br * M + m] = f2bf(red[0][threadIdx.x] + red[1][threadIdx.x] + red[2][threadIdx.x] + red[3][threadIdx.x]);
}

extern "C" void kernel_launch(void* const* d_in, const int* in_sizes, int n_in, void* d_out, int out_size, void* d_ws, size_t ws_size, hipStream_t stream) {
  Ptrs p;
  for (int br = 0; br < 2; ++br) { p.x[br] = (const bf16_t*)d_in[6 * br + 0]; p.ckv[br] = (const bf16_t*)d_in[6 * br + 1]; p.wq[br] = (const bf16_t*)d_in[6 * br + 2];
    p.wuk[br] = (const bf16_t*)d_in[6 * br + 3]; p.wuv[br] = (const bf16_t*)d_in[6 * br + 4]; p.wo[br] = (const bf16_t*)d_in[6 * br + 5]; }
  float* ws = (float*)d_ws;
  float* q = ws; float* qa = q + 2 * B * H * D; float* ml = qa + 2 * B * H * DC; float* ctx = ml + 2 * B * NCH * H * 2; float* op = ctx + 2 * B * H * D;
  k_q<<<dim3(H, 2), 128, 0, stream>>>(p, q);
  k_qa<<<dim3(4, H, 2), 128, 0, stream>>>(p, q, qa);
  k_attn<<<dim3(NCH, B, 2), 256, 0, stream>>>(p, qa, op, ml);
  k_ctx<<<dim3(H, B, 2), 128, 0, stream>>>(p, op, ml, ctx);
  k_out<<<dim3(M / 64, B, 2), 256, 0, stream>>>(p, ctx, (bf16_t*)d_out);
}
```

```cpp
#include <hip/hip_runtime.h>
#include <stdint.h>

#define GAS __attribute__((address_space(1)))
#define LAS __attribute__((address_space(3)))
#define DI __device__ __forceinline__
typedef unsigned short bf16_t;
typedef unsigned v4u __attribute__((ext_vector_type(4)));
typedef unsigned v2u __attribute__((ext_vector_type(2)));
typedef float f32x2 __attribute__((ext_vector_type(2)));
typedef float f32x4 __attribute__((ext_vector_type(4)));
typedef float f32x16 __attribute__((ext_vector_type(16)));
typedef short bf16x8 __attribute__((ext_vector_type(8)));
typedef short s16x4 __attribute__((ext_vector_type(4)));
typedef __bf16 bf16x2_t __attribute__((ext_vector_type(2)));
typedef unsigned long long u64;

namespace mk {

constexpr unsigned WS_EPOCH = 0;
constexpr unsigned WS_C3 = 4096;
constexpr unsigned WS_C4 = WS_C3 + 64 * 256;
constexpr unsigned WS_ML = WS_C4 + 64 * 256;
constexpr unsigned WS_QA = WS_ML + 4 * 32 * 4 * 256;
constexpr unsigned WS_OUTP = WS_QA + 4 * 32 * 2048;
constexpr unsigned WS_OP = WS_OUTP + 128 * 8192;
constexpr unsigned WS_BR = 8u << 20;
static_assert(WS_OP + 32 * 4 * 4 * 32 * 256 <= WS_BR, "ws map");
constexpr unsigned WS_TOTAL = 2 * WS_BR;

constexpr unsigned L_C = 0;
constexpr unsigned L_STG = 127 * 1024;
constexpr unsigned L_P = 131072;
constexpr unsigned L_CC = L_P + 8192;
constexpr unsigned L_CT = L_CC + 4 * 272;
constexpr unsigned L_QP = L_CT;
constexpr unsigned L_FL = 159 * 1024;
constexpr unsigned F_LAND = L_FL + 0;
constexpr unsigned F_QP = L_FL + 16;
constexpr unsigned F_QC = L_FL + 32;
constexpr unsigned F_QS = L_FL + 48;
constexpr unsigned F_QR = L_FL + 64;
constexpr unsigned F_MX = L_FL + 80;
constexpr unsigned F_P = L_FL + 96;
constexpr unsigned F_O = L_FL + 112;
constexpr unsigned F_CC = L_FL + 128;
constexpr unsigned F_CT = L_FL + 144;
constexpr unsigned F_D = L_FL + 160;
constexpr unsigned F_FR = L_FL + 176;
constexpr unsigned L_WB = L_FL + 192;
constexpr unsigned L_FR = L_WB + 256;
constexpr unsigned L_MT = L_CT + 16 * 272;
constexpr unsigned L_M = L_MT + 512;
constexpr unsigned L_LT = L_M + 512;
constexpr unsigned LDS_BYTES = 163840;
static_assert(L_LT + 512 <= L_FL && L_FR + 256 <= LDS_BYTES && (L_MT % 16) == 0 && (L_CT % 16) == 0, "lds map");

constexpr int AUX_SC1 = 16;
constexpr unsigned SENT = 0xFFFFFFFFu;
constexpr float QSCALE = 0.08838834764831845f * 1.4426950408889634f;
#define THR 6
#define BARPOS 0
#define QPLAIN 1
#define PACE 0
#define WOEARLY 1
#define THR2 12
#define THRSW 12
#define UVLAND 32u
#define QSLEEP 8
#define QFIRST 40
#define OSLEEP 4
#define C4POS 1
#define RSLEEP 2
constexpr unsigned SPIN_G = 1u << 16, SPIN_L = 1u << 21;

struct Params {
  const bf16_t* x[2]; const bf16_t* ckv[2]; const bf16_t* wq[2]; const bf16_t* wuk[2]; const bf16_t* wuv[2]; const bf16_t* wo[2];
  unsigned char* ws; bf16_t* out;
};
static_assert(sizeof(Params) == 112, "no padding");

#define RT(k) do { } while (0)

DI __amdgpu_buffer_rsrc_t mkrs(const void* p, unsigned bytes) { return __builtin_amdgcn_make_buffer_rsrc((void*)p, 0, (int)bytes, 0x00020000); }
DI v4u ldb(__amdgpu_buffer_rsrc_t r, unsigned voff, unsigned soff) { return __builtin_amdgcn_raw_buffer_load_b128(r, (int)voff, (int)soff, 0); }
DI f32x4 mfma16(v4u a, v4u b, f32x4 c) { return __builtin_amdgcn_mfma_f32_16x16x32_bf16(__builtin_bit_cast(bf16x8, a), __builtin_bit_cast(bf16x8, b), c, 0, 0, 0); }
DI f32x16 mfma32(v4u a, v4u b, f32x16 c) { return __builtin_amdgcn_mfma_f32_32x32x16_bf16(__builtin_bit_cast(bf16x8, a), __builtin_bit_cast(bf16x8, b), c, 0, 0, 0); }
DI unsigned pk(float lo, float hi) { f32x2 v = {lo, hi}; return __builtin_bit_cast(unsigned, __builtin_convertvector(v, bf16x2_t)); }
DI unsigned unsent(unsigned v) { return v == SENT ? 0x7FFF7FFFu : v; }
DI void cbar() { asm volatile("" ::: "memory"); }
DI void lgkm0() { asm volatile("s_waitcnt lgkmcnt(0)" ::: "memory"); }
DI void vm0() { asm volatile("s_waitcnt vmcnt(0)" ::: "memory"); }
DI v4u tfrag(const v4u* R, int i) {
  const unsigned sel = (i & 1) ? 0x07060302u : 0x05040100u; const int u = i >> 1;
  v4u f;
  f.x = __builtin_amdgcn_perm(R[1][u], R[0][u], sel); f.y = __builtin_amdgcn_perm(R[3][u], R[2][u], sel);
  f.z = __builtin_amdgcn_perm(R[5][u], R[4][u], sel); f.w = __builtin_amdgcn_perm(R[7][u], R[6][u], sel);
  return f;
}
DI void glds16(const void* gsrc, unsigned lds_dst) {
  unsigned keep;
  asm volatile("s_mov_b32 %0, m0\n\ts_mov_b32 m0, %2\n\ts_nop 0\n\tglobal_load_lds_dwordx4 %1, off\n\ts_mov_b32 m0, %0" : "=&s"(keep) : "v"(gsrc), "s"(lds_dst) : "memory");
}
DI v4u lds_ld(LAS unsigned char* L, unsigned off) { return *(LAS v4u*)(L + off); }
DI void lds_st(LAS unsigned char* L, unsigned off, v4u v) { *(LAS v4u*)(L + off) = v; }
DI void lsig(LAS unsigned char* L, unsigned off, unsigned val, int lane) {
  cbar();
  if (lane == 0) *(volatile LAS unsigned*)(L + off) = val;
  cbar();
}
DI bool lge4(LAS unsigned char* L, unsigned off, unsigned want) {
  const v4u f = *(volatile LAS v4u*)(L + off);
  const unsigned m = min(min(f.x, f.y), min(f.z, f.w));
  return (unsigned)__builtin_amdgcn_readfirstlane(m) >= want;
}
DI void lwait4(LAS unsigned char* L, unsigned off, unsigned want) {
  for (unsigned s = 0; s < SPIN_L; ++s) if (lge4(L, off, want)) break;
  cbar();
}
DI void lwait1(LAS unsigned char* L, unsigned off, unsigned want) {
  for (unsigned s = 0; s < SPIN_L; ++s) if ((unsigned)__builtin_amdgcn_readfirstlane(*(volatile LAS unsigned*)(L + off)) >= want) break;
  cbar();
}
template <int CTRL> DI float dpp(float x) { return __builtin_bit_cast(float, __builtin_amdgcn_mov_dpp(__builtin_bit_cast(int, x), CTRL, 0xf, 0xf, true)); }
DI float swap32_max(float x) { auto t = __builtin_amdgcn_permlane32_swap(__float_as_uint(x), __float_as_uint(x), false, false); return fmaxf(__uint_as_float(t[0]), __uint_as_float(t[1])); }
DI float swap16_max(float x) { auto t = __builtin_amdgcn_permlane16_swap(__float_as_uint(x), __float_as_uint(x), false, false); return fmaxf(__uint_as_float(t[0]), __uint_as_float(t[1])); }
DI float swap32_sum(float x) { auto t = __builtin_amdgcn_permlane32_swap(__float_as_uint(x), __float_as_uint(x), false, false); return __uint_as_float(t[0]) + __uint_as_float(t[1]); }
DI float swap16_sum(float x) { auto t = __builtin_amdgcn_permlane16_swap(__float_as_uint(x), __float_as_uint(x), false, false); return __uint_as_float(t[0]) + __uint_as_float(t[1]); }
DI void wait_cnt(unsigned char* p, unsigned want, int lane) {
  for (unsigned sp = 0; sp < SPIN_G; ++sp) {
    unsigned r = 0; const unsigned z = 0;
    if (lane == 0) asm volatile("global_atomic_add %0, %1, %2, off sc0 sc1\n\ts_waitcnt vmcnt(0)" : "=&v"(r) : "v"(p), "v"(z) : "memory");
    if ((unsigned)__builtin_amdgcn_readfirstlane(r) >= want) break;
    __builtin_amdgcn_s_sleep(RSLEEP);
  }
  cbar();
}
DI unsigned umax4(v4u v) { return max(max(v.x, v.y), max(v.z, v.w)); }

DI void pv_load(LAS unsigned char* L, int t, const unsigned (&tb)[8], s16x4 (&a)[16]) {
  const unsigned toff = (unsigned)t * 32768u;
#pragma unroll
  for (int tau = 0; tau < 4; ++tau)
#pragma unroll
    for (int s = 0; s < 2; ++s)
#pragma unroll
      for (int u = 0; u < 2; ++u)
        a[4 * tau + 2 * s + u] = __builtin_amdgcn_ds_read_tr16_b64_v4i16((LAS s16x4*)(L + (tb[2 * tau + u] + toff) + (8192u * u + 16384u * s)));
  __builtin_amdgcn_sched_barrier(0);
}
DI void pv_mma(LAS unsigned char* L, int t, int lane, const s16x4 (&a)[16], float& mprev, f32x16 (&o)[4]) {
  const float mnew = *(LAS float*)(L + L_M + t * 128 + (lane & 31) * 4);
  const v4u p0 = lds_ld(L, L_P + t * 2048 + lane * 16), p1 = lds_ld(L, L_P + t * 2048 + 1024 + lane * 16);
  const float al = __builtin_amdgcn_exp2f(mprev - mnew);
  mprev = mnew;
#pragma unroll
  for (int tau = 0; tau < 4; ++tau) {
    o[tau] *= al;
    o[tau] = mfma32(__builtin_bit_cast(v4u, __builtin_shufflevector(a[4 * tau], a[4 * tau + 1], 0, 1, 2, 3, 4, 5, 6, 7)), p0, o[tau]);
    o[tau] = mfma32(__builtin_bit_cast(v4u, __builtin_shufflevector(a[4 * tau + 2], a[4 * tau + 3], 0, 1, 2, 3, 4, 5, 6, 7)), p1, o[tau]);
  }
}

__global__ void __launch_bounds__(512, 2) mla_fused(Params p) {
  extern __shared__ __attribute__((aligned(16))) unsigned char smem[];
  LAS unsigned char* L = (LAS unsigned char*)smem;
  RT(0);
  const int tid = threadIdx.x, lane = tid & 63, w = __builtin_amdgcn_readfirstlane(tid >> 6);
  const int wg = blockIdx.x, xcd = wg & 7, idx = wg >> 3;
  const int br = xcd >> 2, b = xcd & 3, j = idx;
  const int h = (xcd & 3) * 8 + (idx >> 2), cs = idx & 3;
  const int rank = (xcd & 3) * 32 + idx;
  const int g = lane >> 4, n = lane & 15, hh = lane >> 5, r32 = lane & 31;
  const bf16_t* px = br ? p.x[1] : p.x[0]; const bf16_t* pckv = br ? p.ckv[1] : p.ckv[0]; const bf16_t* pwq = br ? p.wq[1] : p.wq[0];
  const bf16_t* pwuk = br ? p.wuk[1] : p.wuk[0]; const bf16_t* pwuv = br ? p.wuv[1] : p.wuv[0]; const bf16_t* pwo = br ? p.wo[1] : p.wo[0];
  unsigned char* wsb = p.ws + (size_t)br * WS_BR;
  bf16_t* pout = p.out;
  asm volatile("" :: "s"(px), "s"(pckv), "s"(pwq), "s"(pwuk), "s"(pwuv), "s"(pwo), "s"(wsb), "s"(pout));
  const __amdgpu_buffer_rsrc_t rs = mkrs(wsb, WS_BR);

  if (__builtin_expect(w < 4, 1)) {
    const int ww = w;
    __builtin_amdgcn_s_setprio(2);
    v4u xa[4], wqv[32], uk[8];
    const __amdgpu_buffer_rsrc_t rx = mkrs(px, 4096), rq = mkrs(pwq, 4u << 20), rk = mkrs(pwuk, 4u << 20);
    const unsigned vx = (n >> 2) * 1024 + ww * 64 + g * 16;
    const unsigned vq = (ww * 32 + g * 8) * 8192 + h * 256 + n * 16;
    const unsigned vk = (cs * 128 + ww * 32 + n) * 8192 + h * 256 + g * 16;
    const unsigned qo = WS_QA + ((n >> 2) * 32 + h) * 2048 + cs * 512 + ww * 128 + ((n & 1) * 4 + g) * 16;
#pragma unroll
    for (int i = 0; i < 8; ++i) wqv[i] = ldb(rq, vq, (((0 + cs) & 3) * 128 + i) * 8192);
    if ((n & 3) < 2) __builtin_amdgcn_raw_buffer_store_b128(v4u{0u, 0u, 0u, 0u}, rs, qo, 0, AUX_SC1);
    if (rank == 0 && lane < 32) __builtin_amdgcn_raw_buffer_store_b32(0u, rs, WS_C3 + hh * 0 + (lane >> 4) * (64 * 256) + (ww * 16 + n) * 256, 0, AUX_SC1);
#pragma unroll
    for (int q = 0; q < 4; ++q) xa[q] = ldb(rx, vx, ((q + cs) & 3) * 256);
#pragma unroll
    for (int i = 0; i < 4; ++i) uk[i] = ldb(rk, vk + i * 64, 0);
#pragma unroll
    for (int i = 0; i < 4; ++i) uk[4 + i] = ldb(rk, vk + i * 64, 16 * 8192);
#define BAR1(pos) do { if (BARPOS == (pos)) { __builtin_amdgcn_sched_barrier(0); __builtin_amdgcn_s_barrier(); __builtin_amdgcn_sched_barrier(0); } } while (0)
    __builtin_amdgcn_sched_barrier(0);
    BAR1(0);
    RT(1);
    u64 T;
    asm volatile("s_load_dwordx2 %0, %1, 0x0\n\ts_waitcnt lgkmcnt(0)" : "=s"(T) : "s"(wsb) : "memory");
    T += 2;
    f32x4 qa[8];
#pragma unroll
    for (int i = 0; i < 8; ++i) qa[i] = f32x4{0.f, 0.f, 0.f, 0.f};
    asm volatile("" : "+v"(wqv[7]));
    __builtin_amdgcn_sched_barrier(0);
    BAR1(1);
#pragma unroll
    for (int q = 1; q < 4; ++q)
#pragma unroll
      for (int i = 0; i < 8; ++i) wqv[8 * q + i] = ldb(rq, vq, (((q + cs) & 3) * 128 + i) * 8192);
    __builtin_amdgcn_sched_barrier(0);
    BAR1(2);
    RT(15);
    const unsigned Ta = (unsigned)T | 1u;
#pragma unroll
    for (int q = 0; q < 4; ++q)
#pragma unroll
      for (int i = 0; i < 8; ++i) qa[i] = mfma16(xa[q], tfrag(&wqv[8 * q], i), qa[i]);
    {
      v4u v; v.x = pk(qa[0][0], qa[1][0]); v.y = pk(qa[2][0], qa[3][0]); v.z = pk(qa[4][0], qa[5][0]); v.w = pk(qa[6][0], qa[7][0]);
      lds_st(L, L_QP + (4 * g + ww) * 272 + n * 16, v);
    }
    BAR1(3);
    lsig(L, F_QP + 4 * ww, 1u, lane);
    lwait4(L, F_QP, 1u);
    {
      v4u qb[4];
#pragma unroll
      for (int s = 0; s < 4; ++s) qb[s] = lds_ld(L, L_QP + n * 272 + s * 64 + g * 16);
      lsig(L, F_QC + 4 * ww, 1u, lane);
      f32x4 e0 = {0.f, 0.f, 0.f, 0.f}, e1 = {0.f, 0.f, 0.f, 0.f};
#pragma unroll
      for (int s = 0; s < 4; ++s) { e0 = mfma16(uk[s], qb[s], e0); e1 = mfma16(uk[4 + s], qb[s], e1); }
#pragma unroll
      for (int i = 0; i < 4; ++i) {
        e0[i] += dpp<0xB1>(e0[i]); e0[i] += dpp<0x4E>(e0[i]);
        e1[i] += dpp<0xB1>(e1[i]); e1[i] += dpp<0x4E>(e1[i]);
      }
      const int u = n & 3;
      const f32x4 e = (u & 1) ? e1 : e0;
      v4u o; o.x = pk(e[0] * QSCALE, e[1] * QSCALE); o.y = Ta; o.z = pk(e[2] * QSCALE, e[3] * QSCALE); o.w = Ta;
      vm0();
      if (u < 2) __builtin_amdgcn_raw_buffer_store_b128(o, rs, qo, 0, AUX_SC1);
    }
    RT(3);
    v4u qf[32];
    {
      v4u gq[16];
      const unsigned gv = WS_QA + (b * 32 + 8 * ww) * 2048 + lane * 16;
      __builtin_amdgcn_s_sleep(QFIRST);
      cbar();
      RT(2);
#pragma unroll
      for (int i = 0; i < 16; ++i) gq[i] = __builtin_amdgcn_raw_buffer_load_b128(rs, gv + (i & 3) * 1024, (i >> 2) * 4096, 0);
      for (unsigned sp = 0; sp < SPIN_G; ++sp) {
        unsigned bad = 0;
#pragma unroll
        for (int i = 0; i < 16; ++i) bad |= (gq[i].y ^ Ta) | (gq[i].w ^ Ta);
        if (__all(bad == 0u)) break;
        __builtin_amdgcn_s_sleep(QSLEEP);
        cbar();
#pragma unroll
        for (int i = 0; i < 16; ++i) gq[i] = __builtin_amdgcn_raw_buffer_load_b128(rs, gv + (i & 3) * 1024, (i >> 2) * 4096, AUX_SC1);
      }
      cbar();
      RT(4);
      lwait4(L, F_QC, 1u);
#pragma unroll
      for (int i = 0; i < 16; ++i) {
        const unsigned f = (((unsigned)(i >> 1) & 3u) << 2) | ((2u * ww + (unsigned)(i >> 3)) & 3u);
        v2u d; d.x = gq[i].x; d.y = gq[i].z;
        *(LAS v2u*)(L + L_STG + (8 * ww + (i >> 1)) * 1024 + (i & 1) * 512 + ((((unsigned)lane >> 1) ^ f) * 16u) + (lane & 1) * 8) = d;
      }
    }
    lsig(L, F_QS + 4 * ww, 1u, lane);
    lwait4(L, F_QS, 1u);
    const unsigned fr = ((r32 & 3) << 2) | ((r32 >> 2) & 3);
    {
      const unsigned qbase = L_STG + 1024u * r32 + 16u * ((unsigned)hh ^ fr);
#pragma unroll
      for (int i = 0; i < 32; ++i) qf[i] = lds_ld(L, (qbase ^ (32u * (i & 7))) + 256u * (i >> 3));
    }
    lgkm0();
    lsig(L, F_QR + 4 * ww, 1u, lane);
    v4u uv[8];
    const __amdgpu_buffer_rsrc_t rv = mkrs(pwuv, 4u << 20);
    const unsigned vv = (cs * 128 + ww * 32 + g * 8) * 8192 + h * 256 + n * 16;
    const unsigned abase = L_C + 32768u * ww + 1024u * r32 + 16u * ((unsigned)hh ^ fr);
    unsigned ab[8];
#pragma unroll
    for (int s = 0; s < 8; ++s) { ab[s] = abase ^ (32u * s); asm volatile("" : "+v"(ab[s])); }
    lwait4(L, F_LAND, 8u * (ww + 1));
    RT(7);
    f32x16 sp;
#pragma unroll
    for (int i = 0; i < 16; ++i) sp[i] = 0.f;
    {
      v4u af[32];
#pragma unroll
      for (int s = 0; s < 8; ++s) af[s] = lds_ld(L, ab[s]);
      __builtin_amdgcn_sched_barrier(0);
#pragma unroll
      for (int s = 0; s < 32; ++s) {
        if (s + 8 < 32) af[s + 8] = lds_ld(L, ab[s & 7] + 256u * ((s + 8) >> 3));
        sp = mfma32(af[s], qf[s], sp);
        __builtin_amdgcn_sched_barrier(0);
      }
    }
    float mx = fmaxf(fmaxf(sp[0], sp[1]), fmaxf(sp[2], sp[3]));
#pragma unroll
    for (int i = 4; i < 16; i += 2) mx = fmaxf(mx, fmaxf(sp[i], sp[i + 1]));
    float mrun = __builtin_ceilf(swap32_max(mx));
    lwait4(L, F_QR, 1u);
    if (lane < 32) *(LAS float*)(L + L_MT + ww * 128 + lane * 4) = mrun;
    lsig(L, F_MX + 4 * ww, 1u, lane);
    if (ww > 0) {
      for (unsigned s = 0; s < SPIN_L; ++s) {
        const v4u f = *(volatile LAS v4u*)(L + F_MX);
        const unsigned ok = f.x & (ww > 1 ? f.y : 1u) & (ww > 2 ? f.z : 1u);
        if (__builtin_amdgcn_readfirstlane(ok)) break;
      }
      cbar();
      const float m0 = *(LAS float*)(L + L_MT + r32 * 4), m1 = *(LAS float*)(L + L_MT + (ww > 1 ? 128 : 0) + r32 * 4), m2 = *(LAS float*)(L + L_MT + (ww > 2 ? 256 : 0) + r32 * 4);
      mrun = fmaxf(fmaxf(mrun, m0), fmaxf(m1, m2));
    }
    {
      float pe[16];
#pragma unroll
      for (int i = 0; i < 16; ++i) pe[i] = __builtin_amdgcn_exp2f(sp[i] - mrun);
      v4u p0, p1;
      p0.x = pk(pe[0], pe[1]); p0.y = pk(pe[2], pe[3]); p0.z = pk(pe[4], pe[5]); p0.w = pk(pe[6], pe[7]);
      p1.x = pk(pe[8], pe[9]); p1.y = pk(pe[10], pe[11]); p1.z = pk(pe[12], pe[13]); p1.w = pk(pe[14], pe[15]);
      lds_st(L, L_P + ww * 2048 + lane * 16, p0); lds_st(L, L_P + ww * 2048 + 1024 + lane * 16, p1);
      if (lane < 32) *(LAS float*)(L + L_M + ww * 128 + lane * 4) = mrun;
      lsig(L, F_P + 4 * ww, 1u, lane);
      float ls = (((pe[0] + pe[1]) + (pe[2] + pe[3])) + ((pe[4] + pe[5]) + (pe[6] + pe[7]))) + (((pe[8] + pe[9]) + (pe[10] + pe[11])) + ((pe[12] + pe[13]) + (pe[14] + pe[15])));
      ls = swap32_sum(ls);
      if (lane < 32) *(LAS float*)(L + L_LT + ww * 128 + lane * 4) = ls;
      lsig(L, F_P + 4 * ww, 2u, lane);
    }
    RT(8);
    lwait4(L, F_LAND, UVLAND);
#pragma unroll
    for (int i = 0; i < 8; ++i) uv[i] = ldb(rv, vv, i * 8192);
    __builtin_amdgcn_sched_barrier(0);
    lwait4(L, F_P, 2u);
    {
      const float mf = *(LAS float*)(L + L_M + 384 + r32 * 4);
      float lt = *(LAS float*)(L + L_LT + 384 + r32 * 4);
#pragma unroll
      for (int t = 0; t < 3; ++t) lt += *(LAS float*)(L + L_LT + t * 128 + r32 * 4) * __builtin_amdgcn_exp2f(*(LAS float*)(L + L_M + t * 128 + r32 * 4) - mf);
      v2u ml; ml.x = __float_as_uint(mf); ml.y = __float_as_uint(lt);
      if (lane < 32) __builtin_amdgcn_raw_buffer_store_b64(ml, rs, WS_ML + ((b * 32 + j) * 4 + ww) * 256 + lane * 8, 0, AUX_SC1);
    }
    lwait1(L, F_O + 4 * ww, 1u);
    RT(9);
    {
      v4u ov[8];
#pragma unroll
      for (int i = 0; i < 8; ++i) { const int hr = 4 * i + g; ov[i] = lds_ld(L, L_C + 1024u * hr + 256u * ww + 16u * (unsigned)(n ^ (hr & 7))); }
#pragma unroll
      for (int i = 0; i < 8; ++i) __builtin_amdgcn_raw_buffer_store_b128(ov[i], rs, WS_OP + ((b * 32 + j) * 4 + ww) * 8192 + lane * 16 + (i & 3) * 1024, (i >> 2) * 4096, AUX_SC1);
    }
    vm0();
    RT(10);
    if (lane < 4) __hip_atomic_fetch_add((GAS unsigned*)(wsb + WS_C3 + ((ww * 4 + b) * 4 + lane) * 256), 1u, __ATOMIC_RELAXED, __HIP_MEMORY_SCOPE_AGENT);
    lsig(L, F_D + 4 * ww, 1u, lane);
    v4u uvf[8];
#pragma unroll
    for (int i = 0; i < 8; ++i) uvf[i] = tfrag(uv, i);
    wait_cnt(wsb + WS_C3 + ((cs * 4 + ww) * 4 + (h & 3)) * 256, 32u, lane);
    RT(11);
    {
      const v2u rc = __builtin_amdgcn_raw_buffer_load_b64(rs, WS_ML + ((ww * 32 + r32) * 4 + cs) * 256 + h * 8, 0, AUX_SC1);
      v4u ov[8];
      const unsigned oo = WS_OP + ((ww * 32 + g * 8) * 4 + cs) * 8192 + h * 256 + n * 16;
#pragma unroll
      for (int i = 0; i < 8; ++i) ov[i] = __builtin_amdgcn_raw_buffer_load_b128(rs, oo, i * 32768, AUX_SC1);
      __builtin_amdgcn_sched_barrier(0);
      const float mj = __uint_as_float(rc.x), lj = __uint_as_float(rc.y);
      float M = mj;
      M = fmaxf(M, dpp<0xB1>(M)); M = fmaxf(M, dpp<0x4E>(M)); M = fmaxf(M, dpp<0x124>(M)); M = fmaxf(M, dpp<0x128>(M));
      M = swap16_max(M);
      const float ej = __builtin_amdgcn_exp2f(mj - M);
      float Ls = ej * lj;
      Ls += dpp<0xB1>(Ls); Ls += dpp<0x4E>(Ls); Ls += dpp<0x124>(Ls); Ls += dpp<0x128>(Ls);
      Ls = swap16_sum(Ls);
      const float inv = 1.0f / Ls;
      if (lane < 32) *(LAS unsigned short*)(L + L_WB + ww * 64 + lane * 2) = (unsigned short)(__float_as_uint(ej) >> 16);
      cbar();
      const v4u wf = *(volatile LAS v4u*)(L + L_WB + ww * 64 + g * 16);
      float c8[8];
#pragma unroll
      for (int i = 0; i < 8; ++i) { const f32x4 d = mfma16(wf, tfrag(ov, i), f32x4{0.f, 0.f, 0.f, 0.f}); c8[i] = d[0] * inv; }
      v4u v; v.x = pk(c8[0], c8[1]); v.y = pk(c8[2], c8[3]); v.z = pk(c8[4], c8[5]); v.w = pk(c8[6], c8[7]);
      if (lane < 16) lds_st(L, L_CC + ww * 272 + n * 16, v);
    }
    lsig(L, F_CC + 4 * ww, 1u, lane);
    RT(12);
    lwait4(L, F_CC, 1u);
    {
      const v4u af = lds_ld(L, L_CC + (n >> 2) * 272 + ww * 64 + g * 16);
      float c8[8];
#pragma unroll
      for (int i = 0; i < 8; ++i) { const f32x4 d = mfma16(af, uvf[i], f32x4{0.f, 0.f, 0.f, 0.f}); c8[i] = d[0]; }
      v4u v; v.x = pk(c8[0], c8[1]); v.y = pk(c8[2], c8[3]); v.z = pk(c8[4], c8[5]); v.w = pk(c8[6], c8[7]);
      lds_st(L, L_CT + (4 * g + ww) * 272 + n * 16, v);
    }
    lsig(L, F_CT + 4 * ww, 1u, lane);
    RT(13);
    {
      const int u2 = h & 1, bo = (h >> 1) & 3, a4 = h >> 3;
      const unsigned so = WS_OUTP + (ww * 32 + (lane >> 2)) * 8192 + cs * 2048 + u2 * 1024 + (bo * 16 + a4 * 4 + (lane & 3)) * 16;
      v4u pv0, pv1;
      wait_cnt(wsb + WS_C4 + ((cs * 4 + ww) * 4 + (h & 3)) * 256, 32u, lane);
      RT(5);
      for (unsigned sp = 0; sp < SPIN_G; ++sp) {
        cbar();
        pv0 = __builtin_amdgcn_raw_buffer_load_b128(rs, so, 0, AUX_SC1);
        pv1 = __builtin_amdgcn_raw_buffer_load_b128(rs, so, 16 * 8192, AUX_SC1);
        if (__all(max(umax4(pv0), umax4(pv1)) != SENT)) break;
        __builtin_amdgcn_s_sleep(OSLEEP);
      }
      cbar();
      RT(6);
      f32x4 s = __builtin_bit_cast(f32x4, pv0) + __builtin_bit_cast(f32x4, pv1);
#pragma unroll
      for (int i = 0; i < 4; ++i) {
        s[i] += dpp<0x124>(s[i]); s[i] += dpp<0x128>(s[i]);
        s[i] = swap32_sum(swap16_sum(s[i]));
      }
      if (lane < 4) lds_st(L, L_FR + ww * 64 + lane * 16, __builtin_bit_cast(v4u, s));
      lsig(L, F_FR + 4 * ww, 1u, lane);
      if (ww == 0) {
        lwait4(L, F_FR, 1u);
        if (lane < 4) {
          const f32x4 r = (__builtin_bit_cast(f32x4, lds_ld(L, L_FR + lane * 16)) + __builtin_bit_cast(f32x4, lds_ld(L, L_FR + 64 + lane * 16))) +
                          (__builtin_bit_cast(f32x4, lds_ld(L, L_FR + 128 + lane * 16)) + __builtin_bit_cast(f32x4, lds_ld(L, L_FR + 192 + lane * 16)));
          v2u o; o.x = pk(r[0], r[1]); o.y = pk(r[2], r[3]);
          *(GAS v2u*)(pout + bo * 1024 + br * 512 + cs * 128 + (a4 * 4 + lane) * 8 + u2 * 4) = o;
        }
        if (rank == 0 && lane == 0) __hip_atomic_store((GAS u64*)(wsb + WS_EPOCH), T, __ATOMIC_RELAXED, __HIP_MEMORY_SCOPE_AGENT);
      }
    }
  } else {
    const int aw = w - 4;
    if (aw == 1) {
      if (lane < 48) *(volatile LAS unsigned*)(L + L_FL + lane * 4) = 0u;
    }
    lgkm0();
    RT(1);
    __builtin_amdgcn_s_barrier();
    RT(2);
    v4u r127 = {0u, 0u, 0u, 0u};
    {
      const bf16_t* C = pckv + ((size_t)b * 4096 + j * 128) * 512;
#define DMA_ROW(r4_, k_) glds16(C + (size_t)(16 * (r4_) + 4 * (k_) + aw) * 512 + (unsigned)(lane ^ (((unsigned)aw << 2) | (unsigned)(k_))) * 8, \
                                (unsigned)(size_t)smem + L_C + (16 * (r4_) + 4 * (k_) + aw) * 1024)
#pragma unroll
      for (int i = 0; i < 32; ++i) {
        if (i == 31 && aw == 3) r127 = *(const GAS v4u*)(C + (size_t)127 * 512 + (unsigned)(lane ^ 15) * 8);
        else DMA_ROW(i >> 2, i & 3);
        const int win = i < THRSW ? THR : THR2, pwin = (i - 1) < THRSW ? THR : THR2;
        if (win == THR) asm volatile("s_waitcnt vmcnt(%0)" :: "n"(THR) : "memory"); else asm volatile("s_waitcnt vmcnt(%0)" :: "n"(THR2) : "memory");
        int done = i + 1 - win, best = 0;
        for (int k = 0; k < i; ++k) { const int d = k + 1 - (k < THRSW ? THR : THR2); if (d > best) best = d; }
        (void)pwin;
        if (done > 0 && (done >> 3) > (best > 0 ? best >> 3 : 0)) { if (lane == 0) *(volatile LAS unsigned*)(L + F_LAND + 4 * aw) = (unsigned)(done & ~7); }
      }
      if (32 - THR2 < 24) {
        asm volatile("s_waitcnt vmcnt(8)" ::: "memory");
        if (lane == 0) *(volatile LAS unsigned*)(L + F_LAND + 4 * aw) = 24u;
      }
    }
    RT(3);
    unsigned tb[8];
    {
      const unsigned blk = (lane >> 4) & 1, q = (lane & 15) >> 2, pp = lane & 3;
      const unsigned b0 = L_C + 1024u * (4u * hh + q) + 256u * aw + 64u * q + 16u * ((2u * blk + (pp >> 1)) ^ (unsigned)hh) + 8u * (pp & 1);
#pragma unroll
      for (int tau = 0; tau < 4; ++tau) { tb[2 * tau] = b0 ^ (64u * tau); tb[2 * tau + 1] = b0 ^ (64u * tau) ^ 32u; }
    }
    f32x16 o[4];
#pragma unroll
    for (int tau = 0; tau < 4; ++tau)
#pragma unroll
      for (int i = 0; i < 16; ++i) o[tau][i] = 0.f;
    float mprev = -INFINITY;
    int t = 0;
    s16x4 pa[16];
#pragma unroll 1
    while (t < 3 && (unsigned)__builtin_amdgcn_readfirstlane(*(volatile LAS unsigned*)(L + F_P + 4 * t)) >= 1u) { cbar(); pv_load(L, t, tb, pa); pv_mma(L, t, lane, pa, mprev, o); ++t; }
    RT(4);
    vm0();
    if (aw == 3) { lwait4(L, F_QR, 1u); lds_st(L, L_C + 127 * 1024 + lane * 16, r127); cbar(); }
    if (lane == 0) *(volatile LAS unsigned*)(L + F_LAND + 4 * aw) = 32u;
    RT(5);
    v4u wov[32];
    const __amdgpu_buffer_rsrc_t ro = mkrs(pwo, 4u << 20);
    const unsigned vo = (h * 128 + g * 8) * 1024 + aw * 256 + n * 16;
#pragma unroll
    for (int i = 0; i < 8; ++i) wov[i] = ldb(ro, vo, (cs * 32 + i) * 1024);
    __builtin_amdgcn_sched_barrier(0);
#pragma unroll 1
    for (; t < 3; ++t) { lwait4(L, F_LAND, 8u * (t + 1)); pv_load(L, t, tb, pa); lwait1(L, F_P + 4 * t, 1u); pv_mma(L, t, lane, pa, mprev, o); }
    lwait4(L, F_LAND, 32u);
    pv_load(L, 3, tb, pa);
    RT(6);
    lwait1(L, F_P + 12, 1u);
    RT(7);
    pv_mma(L, 3, lane, pa, mprev, o);
#pragma unroll
    for (int tau = 0; tau < 4; ++tau)
#pragma unroll
      for (int v = 0; v < 2; ++v) {
        const unsigned ax = pk(o[tau][8 * v + 0], o[tau][8 * v + 1]), ay = pk(o[tau][8 * v + 2], o[tau][8 * v + 3]);
        const unsigned bx = pk(o[tau][8 * v + 4], o[tau][8 * v + 5]), by = pk(o[tau][8 * v + 6], o[tau][8 * v + 7]);
        auto sx = __builtin_amdgcn_permlane32_swap(ax, bx, false, false);
        auto sy = __builtin_amdgcn_permlane32_swap(ay, by, false, false);
        v4u val; val.x = sx[0]; val.y = sy[0]; val.z = sx[1]; val.w = sy[1];
        const unsigned ch = 4u * tau + 2u * v + (unsigned)hh;
        lds_st(L, L_C + 1024u * r32 + 256u * aw + 16u * (ch ^ (unsigned)(r32 & 7)), val);
      }
    lsig(L, F_O + 4 * aw, 1u, lane);
    RT(8);
    lwait4(L, F_D, 1u);
    {
      const v4u sv = {SENT, SENT, SENT, SENT};
      __builtin_amdgcn_raw_buffer_store_b128(sv, rs, WS_OUTP + rank * 8192 + aw * 2048 + lane * 16, 0, AUX_SC1);
      __builtin_amdgcn_raw_buffer_store_b128(sv, rs, WS_OUTP + rank * 8192 + aw * 2048 + 1024 + lane * 16, 0, AUX_SC1);
    }
#pragma unroll
    for (int q = WOEARLY; q < 4; ++q)
#pragma unroll
      for (int i = 0; i < 8; ++i) wov[8 * q + i] = ldb(ro, vo, (((q + cs) & 3) * 32 + i) * 1024);
    __builtin_amdgcn_sched_barrier(0);
    RT(9);
    v4u wf[32];
#pragma unroll
    for (int q = 0; q < 4; ++q)
#pragma unroll
      for (int i = 0; i < 8; ++i) wf[8 * q + i] = tfrag(&wov[8 * q], i);
#pragma unroll
    for (int i = 0; i < 32; ++i) asm volatile("" : "+v"(wf[i]));
    vm0();
    RT(10);
    lwait4(L, F_CT, 1u);
    RT(11);
#define C4ADD() do { if (lane < 4) __hip_atomic_fetch_add((GAS unsigned*)(wsb + WS_C4 + ((aw * 4 + (rank >> 5)) * 4 + lane) * 256), 1u, __ATOMIC_RELAXED, __HIP_MEMORY_SCOPE_AGENT); } while (0)
    {
      f32x4 d[8];
#pragma unroll
      for (int i = 0; i < 8; ++i) d[i] = f32x4{0.f, 0.f, 0.f, 0.f};
#pragma unroll
      for (int q = 0; q < 4; ++q) {
        const v4u af = lds_ld(L, L_CT + n * 272 + ((q + cs) & 3) * 64 + g * 16);
#pragma unroll
        for (int i = 0; i < 8; ++i) d[i] = mfma16(af, wf[8 * q + i], d[i]);
      }
      __builtin_amdgcn_sched_barrier(0);
      C4ADD();
      __builtin_amdgcn_sched_barrier(0);
      v4u s0, s1;
      unsigned sv[8];
#pragma unroll
      for (int i = 0; i < 8; ++i) { const unsigned x = __float_as_uint((d[i][0] + d[i][1]) + (d[i][2] + d[i][3])); sv[i] = x == SENT ? 0x7FC00000u : x; }
      s0.x = sv[0]; s0.y = sv[1]; s0.z = sv[2]; s0.w = sv[3]; s1.x = sv[4]; s1.y = sv[5]; s1.z = sv[6]; s1.w = sv[7];
      __builtin_amdgcn_raw_buffer_store_b128(s0, rs, WS_OUTP + rank * 8192 + aw * 2048 + lane * 16, 0, AUX_SC1);
      __builtin_amdgcn_raw_buffer_store_b128(s1, rs, WS_OUTP + rank * 8192 + aw * 2048 + 1024 + lane * 16, 0, AUX_SC1);
    }
    RT(12);
  }
}
}

extern "C" void kernel_launch(void* const* d_in, const int* in_sizes, int n_in, void* d_out, int out_size, void* d_ws, size_t ws_size,
                              hipStream_t stream) {
  (void)in_sizes; (void)n_in; (void)out_size; (void)ws_size;
  mk::Params p;
  for (int br = 0; br < 2; ++br) {
    p.x[br] = (const bf16_t*)d_in[6 * br + 0]; p.ckv[br] = (const bf16_t*)d_in[6 * br + 1]; p.wq[br] = (const bf16_t*)d_in[6 * br + 2];
    p.wuk[br] = (const bf16_t*)d_in[6 * br + 3]; p.wuv[br] = (const bf16_t*)d_in[6 * br + 4]; p.wo[br] = (const bf16_t*)d_in[6 * br + 5];
  }
  p.ws = (unsigned char*)d_ws; p.out = (bf16_t*)d_out;
  (void)hipFuncSetAttribute((const void*)mk::mla_fused, hipFuncAttributeMaxDynamicSharedMemorySize, (int)mk::LDS_BYTES);
  mk::mla_fused<<<256, 512, mk::LDS_BYTES, stream>>>(p);
}
```

```cpp
#include <hip/hip_runtime.h>
#include <stdint.h>

#define GAS __attribute__((address_space(1)))
#define LAS __attribute__((address_space(3)))
#define DI __device__ __forceinline__
typedef unsigned short bf16_t;
typedef unsigned v4u __attribute__((ext_vector_type(4)));
typedef unsigned v2u __attribute__((ext_vector_type(2)));
typedef float f32x2 __attribute__((ext_vector_type(2)));
typedef float f32x4 __attribute__((ext_vector_type(4)));
typedef float f32x16 __attribute__((ext_vector_type(16)));
typedef short bf16x8 __attribute__((ext_vector_type(8)));
typedef short s16x4 __attribute__((ext_vector_type(4)));
typedef __bf16 bf16x2_t __attribute__((ext_vector_type(2)));
typedef unsigned long long u64;

namespace mk {

constexpr unsigned WS_EPOCH = 0;
constexpr unsigned WS_C3 = 4096;
constexpr unsigned WS_C4 = WS_C3 + 64 * 256;
constexpr unsigned WS_ML = WS_C4 + 64 * 256;
constexpr unsigned WS_QA = WS_ML + 4 * 32 * 4 * 256;
constexpr unsigned WS_OUTP = WS_QA + 4 * 32 * 2048;
constexpr unsigned WS_OP = WS_OUTP + 128 * 8192;
constexpr unsigned WS_BR = 8u << 20;
static_assert(WS_OP + 32 * 4 * 4 * 32 * 256 <= WS_BR, "ws map");
constexpr unsigned WS_TOTAL = 2 * WS_BR;

constexpr unsigned L_C = 0;
constexpr unsigned L_STG = 127 * 1024;
constexpr unsigned L_P = 131072;
constexpr unsigned L_CC = L_P + 8192;
constexpr unsigned L_CT = L_CC + 4 * 272;
constexpr unsigned L_QP = L_CT;
constexpr unsigned L_FL = 159 * 1024;
constexpr unsigned F_LAND = L_FL + 0;
constexpr unsigned F_QP = L_FL + 16;
constexpr unsigned F_QC = L_FL + 32;
constexpr unsigned F_QS = L_FL + 48;
constexpr unsigned F_QR = L_FL + 64;
constexpr unsigned F_Q0 = L_FL + 80;
constexpr unsigned F_P = L_FL + 96;
constexpr unsigned F_O = L_FL + 112;
constexpr unsigned F_CC = L_FL + 128;
constexpr unsigned F_CT = L_FL + 144;
constexpr unsigned F_D = L_FL + 160;
constexpr unsigned F_FR = L_FL + 176;
constexpr unsigned L_MT = L_FL + 512;
constexpr unsigned L_WB = L_CT + 16 * 272;
constexpr unsigned L_FR = L_WB + 256;
constexpr unsigned L_LT = L_FR + 256;
constexpr unsigned LDS_BYTES = 163840;
static_assert(L_LT + 512 <= L_FL && L_MT + 512 <= LDS_BYTES && (L_MT % 16) == 0 && (L_CT % 16) == 0, "lds map");

constexpr int AUX_SC1 = 16;
constexpr unsigned SENT = 0xFFFFFFFFu;
constexpr float QSCALE = 0.08838834764831845f * 1.4426950408889634f;
#define THR 6
#define APRIO 1
#define TLBPF 1
#define WGATE 100000
#define CODEPF 20
#define BARPOS 0
#define QPLAIN 1
#define PACE 0
#define WOEARLY 1
#define THR2 12
#define THRSW 12
#define UVLAND 32u
#define QSLEEP 8
#define QFIRST 40
#define OSLEEP 4
#define C4POS 1
#define RSLEEP 2
constexpr unsigned SPIN_G = 1u << 16, SPIN_L = 1u << 21;

struct Params {
  const bf16_t* x[2]; const bf16_t* ckv[2]; const bf16_t* wq[2]; const bf16_t* wuk[2]; const bf16_t* wuv[2]; const bf16_t* wo[2];
  unsigned char* ws; bf16_t* out;
};
static_assert(sizeof(Params) == 112, "no padding");

#define RT(k) do { } while (0)

DI __amdgpu_buffer_rsrc_t mkrs(const void* p, unsigned bytes) { return __builtin_amdgcn_make_buffer_rsrc((void*)p, 0, (int)bytes, 0x00020000); }
DI v4u ldb(__amdgpu_buffer_rsrc_t r, unsigned voff, unsigned soff) { return __builtin_amdgcn_raw_buffer_load_b128(r, (int)voff, (int)soff, 0); }
DI f32x4 mfma16(v4u a, v4u b, f32x4 c) { return __builtin_amdgcn_mfma_f32_16x16x32_bf16(__builtin_bit_cast(bf16x8, a), __builtin_bit_cast(bf16x8, b), c, 0, 0, 0); }
DI f32x16 mfma32(v4u a, v4u b, f32x16 c) { return __builtin_amdgcn_mfma_f32_32x32x16_bf16(__builtin_bit_cast(bf16x8, a), __builtin_bit_cast(bf16x8, b), c, 0, 0, 0); }
DI unsigned pk(float lo, float hi) { f32x2 v = {lo, hi}; return __builtin_bit_cast(unsigned, __builtin_convertvector(v, bf16x2_t)); }
DI unsigned unsent(unsigned v) { return v == SENT ? 0x7FFF7FFFu : v; }
DI void cbar() { asm volatile("" ::: "memory"); }
DI void lgkm0() { asm volatile("s_waitcnt lgkmcnt(0)" ::: "memory"); }
DI void vm0() { asm volatile("s_waitcnt vmcnt(0)" ::: "memory"); }
DI v4u tfrag(const v4u* R, int i) {
  const unsigned sel = (i & 1) ? 0x07060302u : 0x05040100u; const int u = i >> 1;
  v4u f;
  f.x = __builtin_amdgcn_perm(R[1][u], R[0][u], sel); f.y = __builtin_amdgcn_perm(R[3][u], R[2][u], sel);
  f.z = __builtin_amdgcn_perm(R[5][u], R[4][u], sel); f.w = __builtin_amdgcn_perm(R[7][u], R[6][u], sel);
  return f;
}
DI void glds16(const void* gsrc, unsigned lds_dst) {
  unsigned keep;
  asm volatile("s_mov_b32 %0, m0\n\ts_mov_b32 m0, %2\n\ts_nop 0\n\tglobal_load_lds_dwordx4 %1, off\n\ts_mov_b32 m0, %0" : "=&s"(keep) : "v"(gsrc), "s"(lds_dst) : "memory");
}
DI v4u lds_ld(LAS unsigned char* L, unsigned off) { return *(LAS v4u*)(L + off); }
DI void lds_st(LAS unsigned char* L, unsigned off, v4u v) { *(LAS v4u*)(L + off) = v; }
DI void lsig(LAS unsigned char* L, unsigned off, unsigned val, int lane) {
  cbar();
  if (lane == 0) *(volatile LAS unsigned*)(L + off) = val;
  cbar();
}
DI bool lge4(LAS unsigned char* L, unsigned off, unsigned want) {
  const v4u f = *(volatile LAS v4u*)(L + off);
  const unsigned m = min(min(f.x, f.y), min(f.z, f.w));
  return (unsigned)__builtin_amdgcn_readfirstlane(m) >= want;
}
DI void lwait4(LAS unsigned char* L, unsigned off, unsigned want) {
  for (unsigned s = 0; s < SPIN_L; ++s) if (lge4(L, off, want)) break;
  cbar();
}
DI void lwait1(LAS unsigned char* L, unsigned off, unsigned want) {
  for (unsigned s = 0; s < SPIN_L; ++s) if ((unsigned)__builtin_amdgcn_readfirstlane(*(volatile LAS unsigned*)(L + off)) >= want) break;
  cbar();
}
template <int CTRL> DI float dpp(float x) { return __builtin_bit_cast(float, __builtin_amdgcn_mov_dpp(__builtin_bit_cast(int, x), CTRL, 0xf, 0xf, true)); }
DI float swap32_max(float x) { auto t = __builtin_amdgcn_permlane32_swap(__float_as_uint(x), __float_as_uint(x), false, false); return fmaxf(__uint_as_float(t[0]), __uint_as_float(t[1])); }
DI float swap16_max(float x) { auto t = __builtin_amdgcn_permlane16_swap(__float_as_uint(x), __float_as_uint(x), false, false); return fmaxf(__uint_as_float(t[0]), __uint_as_float(t[1])); }
DI float swap32_sum(float x) { auto t = __builtin_amdgcn_permlane32_swap(__float_as_uint(x), __float_as_uint(x), false, false); return __uint_as_float(t[0]) + __uint_as_float(t[1]); }
DI float swap16_sum(float x) { auto t = __builtin_amdgcn_permlane16_swap(__float_as_uint(x), __float_as_uint(x), false, false); return __uint_as_float(t[0]) + __uint_as_float(t[1]); }
DI void wait_cnt(unsigned char* p, unsigned want, int lane) {
  for (unsigned sp = 0; sp < SPIN_G; ++sp) {
    unsigned r = 0; const unsigned z = 0;
    if (lane == 0) asm volatile("global_atomic_add %0, %1, %2, off sc0 sc1\n\ts_waitcnt vmcnt(0)" : "=&v"(r) : "v"(p), "v"(z) : "memory");
    if ((unsigned)__builtin_amdgcn_readfirstlane(r) >= want) break;
    __builtin_amdgcn_s_sleep(RSLEEP);
  }
  cbar();
}
DI unsigned umax4(v4u v) { return max(max(v.x, v.y), max(v.z, v.w)); }

DI void pv_load(LAS unsigned char* L, int t, const unsigned (&tb)[8], s16x4 (&a)[16]) {
  const unsigned toff = (unsigned)t * 32768u;
#pragma unroll
  for (int tau = 0; tau < 4; ++tau)
#pragma unroll
    for (int s = 0; s < 2; ++s)
#pragma unroll
      for (int u = 0; u < 2; ++u)
        a[4 * tau + 2 * s + u] = __builtin_amdgcn_ds_read_tr16_b64_v4i16((LAS s16x4*)(L + (tb[2 * tau + u] + toff) + (8192u * u + 16384u * s)));
  __builtin_amdgcn_sched_barrier(0);
}
DI void pv_mma(const s16x4 (&a)[16], v4u p0, v4u p1, f32x16 (&o)[4]) {
#pragma unroll
  for (int tau = 0; tau < 4; ++tau) {
    o[tau] = mfma32(__builtin_bit_cast(v4u, __builtin_shufflevector(a[4 * tau], a[4 * tau + 1], 0, 1, 2, 3, 4, 5, 6, 7)), p0, o[tau]);
    o[tau] = mfma32(__builtin_bit_cast(v4u, __builtin_shufflevector(a[4 * tau + 2], a[4 * tau + 3], 0, 1, 2, 3, 4, 5, 6, 7)), p1, o[tau]);
  }
  __builtin_amdgcn_sched_barrier(0);
}
DI void pv012(LAS unsigned char* L, const unsigned (&tb)[8], int lane, f32x16 (&o)[4]) {
  s16x4 a0[16], a1[16];
  v4u pf[6];
#pragma unroll
  for (int i = 0; i < 6; ++i) pf[i] = lds_ld(L, L_P + i * 1024 + lane * 16);
  pv_load(L, 0, tb, a0);
  pv_load(L, 1, tb, a1);
  pv_mma(a0, pf[0], pf[1], o);
  pv_load(L, 2, tb, a0);
  pv_mma(a1, pf[2], pf[3], o);
  pv_mma(a0, pf[4], pf[5], o);
}
DI bool lge3(LAS unsigned char* L, unsigned off, unsigned want) {
  const v4u f = *(volatile LAS v4u*)(L + off);
  return (unsigned)__builtin_amdgcn_readfirstlane(min(min(f.x, f.y), f.z)) >= want;
}

__global__ void __launch_bounds__(512, 2) mla_fused(Params p) {
  extern __shared__ __attribute__((aligned(16))) unsigned char smem[];
  LAS unsigned char* L = (LAS unsigned char*)smem;
  RT(0);
  const unsigned long long pc0 = __builtin_amdgcn_s_getpc() & ~255ull;
  const int tid = threadIdx.x, lane = tid & 63, w = __builtin_amdgcn_readfirstlane(tid >> 6);
  const int wg = blockIdx.x, xcd = wg & 7, idx = wg >> 3;
  const int br = xcd >> 2, b = xcd & 3, j = idx;
  const int h = (xcd & 3) * 8 + (idx >> 2), cs = idx & 3;
  const int rank = (xcd & 3) * 32 + idx;
  const int g = lane >> 4, n = lane & 15, hh = lane >> 5, r32 = lane & 31;
  const bf16_t* px = br ? p.x[1] : p.x[0]; const bf16_t* pckv = br ? p.ckv[1] : p.ckv[0]; const bf16_t* pwq = br ? p.wq[1] : p.wq[0];
  const bf16_t* pwuk = br ? p.wuk[1] : p.wuk[0]; const bf16_t* pwuv = br ? p.wuv[1] : p.wuv[0]; const bf16_t* pwo = br ? p.wo[1] : p.wo[0];
  unsigned char* wsb = p.ws + (size_t)br * WS_BR;
  bf16_t* pout = p.out;
  asm volatile("" :: "s"(px), "s"(pckv), "s"(pwq), "s"(pwuk), "s"(pwuv), "s"(pwo), "s"(wsb), "s"(pout));
  const __amdgpu_buffer_rsrc_t rs = mkrs(wsb, WS_BR);

  if (__builtin_expect(w < 4, 1)) {
    const int ww = w;
    __builtin_amdgcn_s_setprio(2);
    v4u xa[4], wqv[32], uk[8];
    const __amdgpu_buffer_rsrc_t rx = mkrs(px, 4096), rq = mkrs(pwq, 4u << 20), rk = mkrs(pwuk, 4u << 20);
    const unsigned vx = (n >> 2) * 1024 + ww * 64 + g * 16;
    const unsigned vq = (ww * 32 + g * 8) * 8192 + h * 256 + n * 16;
    const unsigned vk = (cs * 128 + ww * 32 + n) * 8192 + h * 256 + g * 16;
    const unsigned qo = WS_QA + ((n >> 2) * 32 + h) * 2048 + cs * 512 + ww * 128 + ((n & 1) * 4 + g) * 16;
#pragma unroll
    for (int i = 0; i < 8; ++i) wqv[i] = ldb(rq, vq, (((0 + cs) & 3) * 128 + i) * 8192);
    if ((n & 3) < 2) __builtin_amdgcn_raw_buffer_store_b128(v4u{0u, 0u, 0u, 0u}, rs, qo, 0, AUX_SC1);
    if (rank == 0 && lane < 32) __builtin_amdgcn_raw_buffer_store_b32(0u, rs, WS_C3 + hh * 0 + (lane >> 4) * (64 * 256) + (ww * 16 + n) * 256, 0, AUX_SC1);
#pragma unroll
    for (int q = 0; q < 4; ++q) xa[q] = ldb(rx, vx, ((q + cs) & 3) * 256);
#pragma unroll
    for (int i = 0; i < 4; ++i) uk[i] = ldb(rk, vk + i * 64, 0);
#pragma unroll
    for (int i = 0; i < 4; ++i) uk[4 + i] = ldb(rk, vk + i * 64, 16 * 8192);
#define BAR1(pos) do { if (BARPOS == (pos)) { __builtin_amdgcn_sched_barrier(0); __builtin_amdgcn_s_barrier(); __builtin_amdgcn_sched_barrier(0); } } while (0)
    __builtin_amdgcn_sched_barrier(0);
    BAR1(0);
    RT(1);
    u64 T;
    asm volatile("s_load_dwordx2 %0, %1, 0x0\n\ts_waitcnt lgkmcnt(0)" : "=s"(T) : "s"(wsb) : "memory");
    T += 2;
    f32x4 qa[8];
#pragma unroll
    for (int i = 0; i < 8; ++i) qa[i] = f32x4{0.f, 0.f, 0.f, 0.f};
    asm volatile("" : "+v"(wqv[7]));
    __builtin_amdgcn_sched_barrier(0);
    BAR1(1);
#pragma unroll
    for (int q = 1; q < 4; ++q)
#pragma unroll
      for (int i = 0; i < 8; ++i) wqv[8 * q + i] = ldb(rq, vq, (((q + cs) & 3) * 128 + i) * 8192);
    __builtin_amdgcn_sched_barrier(0);
    BAR1(2);
    RT(15);
    const unsigned Ta = (unsigned)T | 1u;
#pragma unroll
    for (int q = 0; q < 4; ++q)
#pragma unroll
      for (int i = 0; i < 8; ++i) qa[i] = mfma16(xa[q], tfrag(&wqv[8 * q], i), qa[i]);
    {
      v4u v; v.x = pk(qa[0][0], qa[1][0]); v.y = pk(qa[2][0], qa[3][0]); v.z = pk(qa[4][0], qa[5][0]); v.w = pk(qa[6][0], qa[7][0]);
      lds_st(L, L_QP + (4 * g + ww) * 272 + n * 16, v);
    }
    BAR1(3);
    lsig(L, F_QP + 4 * ww, 1u, lane);
    lwait4(L, F_QP, 1u);
    {
      v4u qb[4];
#pragma unroll
      for (int s = 0; s < 4; ++s) qb[s] = lds_ld(L, L_QP + n * 272 + s * 64 + g * 16);
      lsig(L, F_QC + 4 * ww, 1u, lane);
      f32x4 e0 = {0.f, 0.f, 0.f, 0.f}, e1 = {0.f, 0.f, 0.f, 0.f};
#pragma unroll
      for (int s = 0; s < 4; ++s) { e0 = mfma16(uk[s], qb[s], e0); e1 = mfma16(uk[4 + s], qb[s], e1); }
#pragma unroll
      for (int i = 0; i < 4; ++i) {
        e0[i] += dpp<0xB1>(e0[i]); e0[i] += dpp<0x4E>(e0[i]);
        e1[i] += dpp<0xB1>(e1[i]); e1[i] += dpp<0x4E>(e1[i]);
      }
      const int u = n & 3;
      const f32x4 e = (u & 1) ? e1 : e0;
      v4u o; o.x = pk(e[0] * QSCALE, e[1] * QSCALE); o.y = Ta; o.z = pk(e[2] * QSCALE, e[3] * QSCALE); o.w = Ta;
      vm0();
      if (u < 2) __builtin_amdgcn_raw_buffer_store_b128(o, rs, qo, 0, AUX_SC1);
    }
    RT(3);
    v4u qf[32];
    {
      v4u gq[16];
      const unsigned gv = WS_QA + (b * 32 + 8 * ww) * 2048 + lane * 16;
      __builtin_amdgcn_s_sleep(QFIRST);
      cbar();
      RT(2);
#pragma unroll
      for (int i = 0; i < 16; ++i) gq[i] = __builtin_amdgcn_raw_buffer_load_b128(rs, gv + (i & 3) * 1024, (i >> 2) * 4096, 0);
      for (unsigned sp = 0; sp < SPIN_G; ++sp) {
        unsigned bad = 0;
#pragma unroll
        for (int i = 0; i < 16; ++i) bad |= (gq[i].y ^ Ta) | (gq[i].w ^ Ta);
        if (__all(bad == 0u)) break;
        __builtin_amdgcn_s_sleep(QSLEEP);
        cbar();
#pragma unroll
        for (int i = 0; i < 16; ++i) gq[i] = __builtin_amdgcn_raw_buffer_load_b128(rs, gv + (i & 3) * 1024, (i >> 2) * 4096, AUX_SC1);
      }
      cbar();
      RT(4);
      lwait4(L, F_QC, 1u);
      const unsigned sx = ((unsigned)(lane >> 2) & 3u) | (((unsigned)(lane >> 1) & 1u) << 2);
      const unsigned wb = L_STG + (unsigned)(lane >> 2) * 1024u + (((unsigned)lane >> 1) & 1u) * 512u + (lane & 1) * 8u + (unsigned)ww * 128u;
#pragma unroll
      for (int i = 0; i < 16; ++i) {
        v2u d; d.x = gq[i].x; d.y = gq[i].z;
        *(LAS v2u*)(L + wb + (i & 1) * 16384 + (((unsigned)(i >> 1) ^ sx) * 16u)) = d;
      }
    }
    lsig(L, F_QS + 4 * ww, 1u, lane);
    const unsigned fr = ((r32 & 3) << 2) | ((r32 >> 2) & 3);
    const unsigned rowb = 1024u * r32 + 16u * ((unsigned)hh ^ fr);
    unsigned ab[8], qb4[4];
#pragma unroll
    for (int s = 0; s < 8; ++s) { ab[s] = (L_C + 32768u * ww + rowb) ^ (32u * s); asm volatile("" : "+v"(ab[s])); }
#pragma unroll
    for (int s = 0; s < 4; ++s) { qb4[s] = L_STG + 512u * hh + (((unsigned)r32 ^ ((unsigned)s | ((unsigned)hh << 2))) * 16u); asm volatile("" : "+v"(qb4[s])); }
    lwait4(L, F_QS, 1u);
    f32x16 sp;
#pragma unroll
    for (int i = 0; i < 16; ++i) sp[i] = 0.f;
    if (lge4(L, F_LAND, 8u * (ww + 1))) {
      cbar();
      RT(7);
      v4u af[32];
#pragma unroll
      for (int s = 0; s < 6; ++s) { qf[s] = lds_ld(L, qb4[s & 3] + 1024u * s); af[s] = lds_ld(L, ab[s]); }
      __builtin_amdgcn_sched_barrier(0);
#pragma unroll
      for (int s = 0; s < 32; ++s) {
        if (s + 6 < 32) { qf[s + 6] = lds_ld(L, qb4[(s + 6) & 3] + 1024u * (s + 6)); af[s + 6] = lds_ld(L, ab[(s + 6) & 7] + 256u * ((s + 6) >> 3)); }
        sp = mfma32(af[s], qf[s], sp);
        if (s == 0) lsig(L, F_Q0 + 4 * ww, 1u, lane);
        __builtin_amdgcn_sched_barrier(0);
      }
      lsig(L, F_QR + 4 * ww, 1u, lane);
    } else {
      qf[0] = lds_ld(L, qb4[0]);
      lsig(L, F_Q0 + 4 * ww, 1u, lane);
#pragma unroll
      for (int i = 1; i < 32; ++i) qf[i] = lds_ld(L, qb4[i & 3] + 1024u * i);
      lgkm0();
      lsig(L, F_QR + 4 * ww, 1u, lane);
      lwait4(L, F_LAND, 8u * (ww + 1));
      RT(7);
      v4u af[32];
#pragma unroll
      for (int s = 0; s < 8; ++s) af[s] = lds_ld(L, ab[s]);
      __builtin_amdgcn_sched_barrier(0);
#pragma unroll
      for (int s = 0; s < 32; ++s) {
        if (s + 8 < 32) af[s + 8] = lds_ld(L, ab[s & 7] + 256u * ((s + 8) >> 3));
        sp = mfma32(af[s], qf[s], sp);
        __builtin_amdgcn_sched_barrier(0);
      }
    }
    {
      float mx = fmaxf(fmaxf(sp[0], sp[1]), fmaxf(sp[2], sp[3]));
#pragma unroll
      for (int i = 4; i < 16; i += 2) mx = fmaxf(mx, fmaxf(sp[i], sp[i + 1]));
      float mrun = __builtin_ceilf(swap32_max(mx));
      { const unsigned mb = __float_as_uint(mrun); if (lane < 32) *(volatile LAS unsigned*)(L + L_MT + lane * 16 + ww * 4) = mb == SENT ? 0x7FC00000u : mb; }
      v4u mt;
      for (unsigned s = 0; s < SPIN_L; ++s) {
        mt = *(volatile LAS v4u*)(L + L_MT + r32 * 16);
        if (__all(max(max(mt.x, mt.y), mt.z) != SENT)) break;
      }
      mrun = fmaxf(fmaxf(mrun, __uint_as_float(mt.x)), fmaxf(__uint_as_float(mt.y), __uint_as_float(mt.z)));
      float pe[16];
#pragma unroll
      for (int i = 0; i < 16; ++i) pe[i] = __builtin_amdgcn_exp2f(sp[i] - mrun);
      v4u p0, p1;
      p0.x = pk(pe[0], pe[1]); p0.y = pk(pe[2], pe[3]); p0.z = pk(pe[4], pe[5]); p0.w = pk(pe[6], pe[7]);
      p1.x = pk(pe[8], pe[9]); p1.y = pk(pe[10], pe[11]); p1.z = pk(pe[12], pe[13]); p1.w = pk(pe[14], pe[15]);
      lwait4(L, F_QR, 1u);
      lds_st(L, L_P + ww * 2048 + lane * 16, p0); lds_st(L, L_P + ww * 2048 + 1024 + lane * 16, p1);
      lsig(L, F_P + 4 * ww, 1u, lane);
      float ls = (((pe[0] + pe[1]) + (pe[2] + pe[3])) + ((pe[4] + pe[5]) + (pe[6] + pe[7]))) + (((pe[8] + pe[9]) + (pe[10] + pe[11])) + ((pe[12] + pe[13]) + (pe[14] + pe[15])));
      ls = swap32_sum(ls);
      if (lane < 32) *(LAS float*)(L + L_LT + lane * 16 + ww * 4) = ls;
      lsig(L, F_P + 4 * ww, 2u, lane);
    }
    RT(8);
    wait_cnt(wsb + WS_C3 + ((cs * 4 + ww) * 4 + (h & 3)) * 256, 32u, lane);
    RT(11);
    {
      const v2u rc = __builtin_amdgcn_raw_buffer_load_b64(rs, WS_ML + ((ww * 32 + r32) * 4 + cs) * 256 + h * 8, 0, AUX_SC1);
      v4u ov[8];
      const unsigned oo = WS_OP + ((ww * 32 + g * 8) * 4 + cs) * 8192 + h * 256 + n * 16;
#pragma unroll
      for (int i = 0; i < 8; ++i) ov[i] = __builtin_amdgcn_raw_buffer_load_b128(rs, oo, i * 32768, AUX_SC1);
      __builtin_amdgcn_sched_barrier(0);
      const float mj = __uint_as_float(rc.x), lj = __uint_as_float(rc.y);
      float M = mj;
      M = fmaxf(M, dpp<0xB1>(M)); M = fmaxf(M, dpp<0x4E>(M)); M = fmaxf(M, dpp<0x124>(M)); M = fmaxf(M, dpp<0x128>(M));
      M = swap16_max(M);
      const float ej = __builtin_amdgcn_exp2f(mj - M);
      float Ls = ej * lj;
      Ls += dpp<0xB1>(Ls); Ls += dpp<0x4E>(Ls); Ls += dpp<0x124>(Ls); Ls += dpp<0x128>(Ls);
      Ls = swap16_sum(Ls);
      const float inv = 1.0f / Ls;
      if (lane < 32) *(LAS unsigned short*)(L + L_WB + ww * 64 + lane * 2) = (unsigned short)(__float_as_uint(ej) >> 16);
      cbar();
      const v4u wf = *(volatile LAS v4u*)(L + L_WB + ww * 64 + g * 16);
      float c8[8];
#pragma unroll
      for (int i = 0; i < 8; ++i) { const f32x4 d = mfma16(wf, tfrag(ov, i), f32x4{0.f, 0.f, 0.f, 0.f}); c8[i] = d[0] * inv; }
      v4u v; v.x = pk(c8[0], c8[1]); v.y = pk(c8[2], c8[3]); v.z = pk(c8[4], c8[5]); v.w = pk(c8[6], c8[7]);
      if (lane < 16) lds_st(L, L_CC + ww * 272 + n * 16, v);
    }
    lsig(L, F_CC + 4 * ww, 1u, lane);
    RT(12);
    if (ww == 0) {
      const int u2 = h & 1, bo = (h >> 1) & 3, a4 = h >> 3;
      const unsigned so = WS_OUTP + (lane >> 2) * 8192 + cs * 2048 + u2 * 1024 + (bo * 16 + a4 * 4 + (lane & 3)) * 16;
      {
        unsigned char* cp = wsb + WS_C4 + ((cs * 4 + (lane & 3)) * 4 + (h & 3)) * 256;
        for (unsigned sp = 0; sp < SPIN_G; ++sp) {
          unsigned r = 32u; const unsigned z = 0;
          if (lane < 4) asm volatile("global_atomic_add %0, %1, %2, off sc0 sc1\n\ts_waitcnt vmcnt(0)" : "=&v"(r) : "v"(cp), "v"(z) : "memory");
          if (__all(r >= 32u)) break;
          __builtin_amdgcn_s_sleep(RSLEEP);
        }
        cbar();
      }
      RT(13);
      v4u pv[8];
      for (unsigned sp = 0; sp < SPIN_G; ++sp) {
        cbar();
#pragma unroll
        for (int i = 0; i < 8; ++i) pv[i] = __builtin_amdgcn_raw_buffer_load_b128(rs, so, i * (16 * 8192), AUX_SC1);
        unsigned m = 0;
#pragma unroll
        for (int i = 0; i < 8; ++i) m = max(m, umax4(pv[i]));
        if (__all(m != SENT)) break;
        __builtin_amdgcn_s_sleep(OSLEEP);
      }
      cbar();
      RT(5);
      f32x4 s = ((__builtin_bit_cast(f32x4, pv[0]) + __builtin_bit_cast(f32x4, pv[1])) + (__builtin_bit_cast(f32x4, pv[2]) + __builtin_bit_cast(f32x4, pv[3]))) +
                ((__builtin_bit_cast(f32x4, pv[4]) + __builtin_bit_cast(f32x4, pv[5])) + (__builtin_bit_cast(f32x4, pv[6]) + __builtin_bit_cast(f32x4, pv[7])));
#pragma unroll
      for (int i = 0; i < 4; ++i) {
        s[i] += dpp<0x124>(s[i]); s[i] += dpp<0x128>(s[i]);
        s[i] = swap32_sum(swap16_sum(s[i]));
      }
      if (lane < 4) {
        v2u o2; o2.x = pk(s[0], s[1]); o2.y = pk(s[2], s[3]);
        *(GAS v2u*)(pout + bo * 1024 + br * 512 + cs * 128 + (a4 * 4 + lane) * 8 + u2 * 4) = o2;
      }
      if (rank == 0 && lane == 0) __hip_atomic_store((GAS u64*)(wsb + WS_EPOCH), T, __ATOMIC_RELAXED, __HIP_MEMORY_SCOPE_AGENT);
      RT(6);
    }
  } else {
    const int aw = w - 4;
    const unsigned tk0 = (unsigned)__builtin_amdgcn_s_memrealtime();
    unsigned tpf = 0;
    if (aw == 3) {
      const bf16_t* a = lane == 0 ? pckv + ((size_t)b * 4096 + j * 128) * 512 : lane == 1 ? pwuv + (size_t)(cs * 128) * 4096 + h * 128 : lane == 2 ? pwo + (size_t)h * 128 * 512
                        : lane == 3 ? pwuv + (size_t)(cs * 128 + 127) * 4096 + h * 128 : pckv + ((size_t)b * 4096 + j * 128 + 127) * 512;
      if (lane < 5) tpf = *(const GAS unsigned*)a;
    }
    v4u cpf = {0u, 0u, 0u, 0u};
    if (aw == 2 && idx < CODEPF) cpf = *(const GAS v4u*)(pc0 + (size_t)idx * 1024 + lane * 16);
    if (aw == 1) {
      if (lane < 48) *(volatile LAS unsigned*)(L + L_FL + lane * 4) = 0u;
      *(volatile LAS v2u*)(L + L_MT + lane * 8) = v2u{SENT, SENT};
    }
    lgkm0();
    RT(1);
    __builtin_amdgcn_s_barrier();
    asm volatile("" :: "v"(cpf));
    asm volatile("" :: "v"(tpf));
    RT(2);
    v4u r127 = {0u, 0u, 0u, 0u};
    {
      const bf16_t* C = pckv + ((size_t)b * 4096 + j * 128) * 512;
#define DMA_ROW(r4_, k_) glds16(C + (size_t)(16 * (r4_) + 4 * (k_) + aw) * 512 + (unsigned)(lane ^ (((unsigned)aw << 2) | (unsigned)(k_))) * 8, \
                                (unsigned)(size_t)smem + L_C + (16 * (r4_) + 4 * (k_) + aw) * 1024)
#pragma unroll
      for (int i = 0; i < 32; ++i) {
        if (i == 31 && aw == 3) r127 = *(const GAS v4u*)(C + (size_t)127 * 512 + (unsigned)(lane ^ 15) * 8);
        else DMA_ROW(i >> 2, i & 3);
        const int win = i < THRSW ? THR : THR2, pwin = (i - 1) < THRSW ? THR : THR2;
        if (win == THR) asm volatile("s_waitcnt vmcnt(%0)" :: "n"(THR) : "memory"); else asm volatile("s_waitcnt vmcnt(%0)" :: "n"(THR2) : "memory");
        int done = i + 1 - win, best = 0;
        for (int k = 0; k < i; ++k) { const int d = k + 1 - (k < THRSW ? THR : THR2); if (d > best) best = d; }
        (void)pwin;
        if (done > 0 && (done >> 3) > (best > 0 ? best >> 3 : 0)) { if (lane == 0) *(volatile LAS unsigned*)(L + F_LAND + 4 * aw) = (unsigned)(done & ~7); }
      }
      if (32 - THR2 < 24) {
        asm volatile("s_waitcnt vmcnt(8)" ::: "memory");
        if (lane == 0) *(volatile LAS unsigned*)(L + F_LAND + 4 * aw) = 24u;
      }
    }
    RT(3);
    unsigned tb[8];
    {
      const unsigned blk = (lane >> 4) & 1, q = (lane & 15) >> 2, pp = lane & 3;
      const unsigned b0 = L_C + 1024u * (4u * hh + q) + 256u * aw + 64u * q + 16u * ((2u * blk + (pp >> 1)) ^ (unsigned)hh) + 8u * (pp & 1);
#pragma unroll
      for (int tau = 0; tau < 4; ++tau) { tb[2 * tau] = b0 ^ (64u * tau); tb[2 * tau + 1] = b0 ^ (64u * tau) ^ 32u; }
    }
    f32x16 o[4];
#pragma unroll
    for (int tau = 0; tau < 4; ++tau)
#pragma unroll
      for (int i = 0; i < 16; ++i) o[tau][i] = 0.f;
    __builtin_amdgcn_s_setprio(APRIO);
    const bool early = lge3(L, F_P, 1u);
    if (early) { cbar(); pv012(L, tb, lane, o); }
    RT(4);
    vm0();
    if (aw == 3) { lwait4(L, F_Q0, 1u); lds_st(L, L_C + 127 * 1024 + lane * 16, r127); cbar(); }
    if (lane == 0) *(volatile LAS unsigned*)(L + F_LAND + 4 * aw) = 32u;
    RT(5);
    v4u uv[8], wov[32];
    const __amdgpu_buffer_rsrc_t rv = mkrs(pwuv, 4u << 20), ro = mkrs(pwo, 4u << 20);
    const unsigned vv = (cs * 128 + aw * 32 + g * 8) * 8192 + h * 256 + n * 16;
    const unsigned vo = (h * 128 + g * 8) * 1024 + aw * 256 + n * 16;
    const bool wnow = !early && (int)((unsigned)__builtin_amdgcn_s_memrealtime() - tk0) < WGATE;
    if (wnow) {
#pragma unroll
      for (int i = 0; i < 8; ++i) uv[i] = ldb(rv, vv, i * 8192);
#pragma unroll
      for (int i = 0; i < 8; ++i) wov[i] = ldb(ro, vo, (cs * 32 + i) * 1024);
    }
    __builtin_amdgcn_sched_barrier(0);
    if (!early) {
      for (unsigned s = 0; s < SPIN_L; ++s) if (lge3(L, F_P, 1u)) break;
      cbar();
      pv012(L, tb, lane, o);
    }
    {
      s16x4 pa[16];
      lwait4(L, F_LAND, 32u);
      pv_load(L, 3, tb, pa);
      RT(6);
      lwait1(L, F_P + 12, 1u);
      RT(7);
      const f32x4 mt = *(LAS f32x4*)(L + L_MT + r32 * 16);
      const v4u p0 = lds_ld(L, L_P + 6144 + lane * 16), p1 = lds_ld(L, L_P + 7168 + lane * 16);
      const float m012 = fmaxf(fmaxf(mt[0], mt[1]), mt[2]), mf = fmaxf(m012, mt[3]);
      const float al = __builtin_amdgcn_exp2f(m012 - mf);
#pragma unroll
      for (int tau = 0; tau < 4; ++tau) {
        o[tau] *= al;
        o[tau] = mfma32(__builtin_bit_cast(v4u, __builtin_shufflevector(pa[4 * tau], pa[4 * tau + 1], 0, 1, 2, 3, 4, 5, 6, 7)), p0, o[tau]);
        o[tau] = mfma32(__builtin_bit_cast(v4u, __builtin_shufflevector(pa[4 * tau + 2], pa[4 * tau + 3], 0, 1, 2, 3, 4, 5, 6, 7)), p1, o[tau]);
      }
#pragma unroll
      for (int tau = 0; tau < 4; ++tau)
#pragma unroll
        for (int v = 0; v < 2; ++v) {
          const unsigned ax = pk(o[tau][8 * v + 0], o[tau][8 * v + 1]), ay = pk(o[tau][8 * v + 2], o[tau][8 * v + 3]);
          const unsigned bx = pk(o[tau][8 * v + 4], o[tau][8 * v + 5]), by = pk(o[tau][8 * v + 6], o[tau][8 * v + 7]);
          auto sx = __builtin_amdgcn_permlane32_swap(ax, bx, false, false);
          auto sy = __builtin_amdgcn_permlane32_swap(ay, by, false, false);
          v4u val; val.x = sx[0]; val.y = sy[0]; val.z = sx[1]; val.w = sy[1];
          const unsigned ch = 4u * tau + 2u * v + (unsigned)hh;
          lds_st(L, L_C + 1024u * r32 + 256u * aw + 16u * (ch ^ (unsigned)(r32 & 7)), val);
        }
      cbar();
      RT(8);
      v4u ov[8];
#pragma unroll
      for (int i = 0; i < 8; ++i) { const int hr = 4 * i + g; ov[i] = lds_ld(L, L_C + 1024u * hr + 256u * aw + 16u * (unsigned)(n ^ (hr & 7))); }
#pragma unroll
      for (int i = 0; i < 8; ++i) __builtin_amdgcn_raw_buffer_store_b128(ov[i], rs, WS_OP + ((b * 32 + j) * 4 + aw) * 8192 + lane * 16 + (i & 3) * 1024, (i >> 2) * 4096, AUX_SC1);
      lwait4(L, F_P, 2u);
      const f32x4 lt = *(LAS f32x4*)(L + L_LT + r32 * 16);
      v2u ml; ml.x = __float_as_uint(mf); ml.y = __float_as_uint(((lt[0] + lt[1]) + lt[2]) * al + lt[3]);
      if (lane < 32) __builtin_amdgcn_raw_buffer_store_b64(ml, rs, WS_ML + ((b * 32 + j) * 4 + aw) * 256 + lane * 8, 0, AUX_SC1);
    }
    RT(9);
    vm0();
    RT(10);
    if (lane < 4) __hip_atomic_fetch_add((GAS unsigned*)(wsb + WS_C3 + ((aw * 4 + b) * 4 + lane) * 256), 1u, __ATOMIC_RELAXED, __HIP_MEMORY_SCOPE_AGENT);
    if (!wnow) {
#pragma unroll
      for (int i = 0; i < 8; ++i) uv[i] = ldb(rv, vv, i * 8192);
#pragma unroll
      for (int i = 0; i < 8; ++i) wov[i] = ldb(ro, vo, (cs * 32 + i) * 1024);
    }
    {
      const v4u sv = {SENT, SENT, SENT, SENT};
      __builtin_amdgcn_raw_buffer_store_b128(sv, rs, WS_OUTP + rank * 8192 + aw * 2048 + lane * 16, 0, AUX_SC1);
      __builtin_amdgcn_raw_buffer_store_b128(sv, rs, WS_OUTP + rank * 8192 + aw * 2048 + 1024 + lane * 16, 0, AUX_SC1);
    }
#pragma unroll
    for (int q = 1; q < 4; ++q)
#pragma unroll
      for (int i = 0; i < 8; ++i) wov[8 * q + i] = ldb(ro, vo, (((q + cs) & 3) * 32 + i) * 1024);
    __builtin_amdgcn_sched_barrier(0);
    v4u uvf[8], wf[32];
#pragma unroll
    for (int i = 0; i < 8; ++i) uvf[i] = tfrag(uv, i);
#pragma unroll
    for (int q = 0; q < 4; ++q)
#pragma unroll
      for (int i = 0; i < 8; ++i) wf[8 * q + i] = tfrag(&wov[8 * q], i);
#pragma unroll
    for (int i = 0; i < 8; ++i) asm volatile("" : "+v"(uvf[i]));
#pragma unroll
    for (int i = 0; i < 32; ++i) asm volatile("" : "+v"(wf[i]));
    vm0();
    RT(11);
    lwait4(L, F_CC, 1u);
    RT(12);
    {
      const v4u af = lds_ld(L, L_CC + (n >> 2) * 272 + aw * 64 + g * 16);
      float c8[8];
#pragma unroll
      for (int i = 0; i < 8; ++i) { const f32x4 d = mfma16(af, uvf[i], f32x4{0.f, 0.f, 0.f, 0.f}); c8[i] = d[0]; }
      v4u v; v.x = pk(c8[0], c8[1]); v.y = pk(c8[2], c8[3]); v.z = pk(c8[4], c8[5]); v.w = pk(c8[6], c8[7]);
      lds_st(L, L_CT + (4 * g + aw) * 272 + n * 16, v);
    }
    lsig(L, F_CT + 4 * aw, 1u, lane);
    lwait4(L, F_CT, 1u);
    RT(14);
#define C4ADD() do { if (lane < 4) __hip_atomic_fetch_add((GAS unsigned*)(wsb + WS_C4 + ((aw * 4 + (rank >> 5)) * 4 + lane) * 256), 1u, __ATOMIC_RELAXED, __HIP_MEMORY_SCOPE_AGENT); } while (0)
    {
      v4u af[4];
#pragma unroll
      for (int q = 0; q < 4; ++q) af[q] = lds_ld(L, L_CT + n * 272 + ((q + cs) & 3) * 64 + g * 16);
      f32x4 d[8];
#pragma unroll
      for (int i = 0; i < 8; ++i) d[i] = f32x4{0.f, 0.f, 0.f, 0.f};
#pragma unroll
      for (int q = 0; q < 4; ++q)
#pragma unroll
        for (int i = 0; i < 8; ++i) d[i] = mfma16(af[q], wf[8 * q + i], d[i]);
      __builtin_amdgcn_sched_barrier(0);
      C4ADD();
      __builtin_amdgcn_sched_barrier(0);
      v4u s0, s1;
      unsigned sv[8];
#pragma unroll
      for (int i = 0; i < 8; ++i) { const unsigned x = __float_as_uint((d[i][0] + d[i][1]) + (d[i][2] + d[i][3])); sv[i] = x == SENT ? 0x7FC00000u : x; }
      s0.x = sv[0]; s0.y = sv[1]; s0.z = sv[2]; s0.w = sv[3]; s1.x = sv[4]; s1.y = sv[5]; s1.z = sv[6]; s1.w = sv[7];
      __builtin_amdgcn_raw_buffer_store_b128(s0, rs, WS_OUTP + rank * 8192 + aw * 2048 + lane * 16, 0, AUX_SC1);
      __builtin_amdgcn_raw_buffer_store_b128(s1, rs, WS_OUTP + rank * 8192 + aw * 2048 + 1024 + lane * 16, 0, AUX_SC1);
    }
    RT(13);
  }
}
}

extern "C" void kernel_launch(void* const* d_in, const int* in_sizes, int n_in, void* d_out, int out_size, void* d_ws, size_t ws_size,
                              hipStream_t stream) {
  (void)in_sizes; (void)n_in; (void)out_size; (void)ws_size;
  mk::Params p;
  for (int br = 0; br < 2; ++br) {
    p.x[br] = (const bf16_t*)d_in[6 * br + 0]; p.ckv[br] = (const bf16_t*)d_in[6 * br + 1]; p.wq[br] = (const bf16_t*)d_in[6 * br + 2];
    p.wuk[br] = (const bf16_t*)d_in[6 * br + 3]; p.wuv[br] = (const bf16_t*)d_in[6 * br + 4]; p.wo[br] = (const bf16_t*)d_in[6 * br + 5];
  }
  p.ws = (unsigned char*)d_ws; p.out = (bf16_t*)d_out;
  (void)hipFuncSetAttribute((const void*)mk::mla_fused, hipFuncAttributeMaxDynamicSharedMemorySize, (int)mk::LDS_BYTES);
  mk::mla_fused<<<256, 512, mk::LDS_BYTES, stream>>>(p);
}
```

```cpp
#include <hip/hip_runtime.h>
#include <stdint.h>

#define GAS __attribute__((address_space(1)))
#define LAS __attribute__((address_space(3)))
#define DI __device__ __forceinline__
typedef unsigned short bf16_t;
typedef unsigned v4u __attribute__((ext_vector_type(4)));
typedef unsigned v2u __attribute__((ext_vector_type(2)));
typedef float f32x2 __attribute__((ext_vector_type(2)));
typedef float f32x4 __attribute__((ext_vector_type(4)));
typedef float f32x16 __attribute__((ext_vector_type(16)));
typedef short bf16x8 __attribute__((ext_vector_type(8)));
typedef short s16x4 __attribute__((ext_vector_type(4)));
typedef __bf16 bf16x2_t __attribute__((ext_vector_type(2)));
typedef unsigned long long u64;

namespace mk {

constexpr unsigned WS_EPOCH = 0;
constexpr unsigned WS_C3 = 4096;
constexpr unsigned WS_C4 = WS_C3 + 64 * 256;
constexpr unsigned WS_ML = WS_C4 + 64 * 256;
constexpr unsigned WS_QA = WS_ML + 4 * 32 * 4 * 256;
constexpr unsigned WS_OUTP = WS_QA + 4 * 32 * 2048;
constexpr unsigned WS_OP = WS_OUTP + 128 * 8192;
constexpr unsigned WS_BR = 8u << 20;
static_assert(WS_OP + 32 * 4 * 4 * 32 * 256 <= WS_BR, "ws map");
constexpr unsigned WS_TOTAL = 2 * WS_BR;

constexpr unsigned L_C = 0;
constexpr unsigned L_STG = 127 * 1024;
constexpr unsigned L_P = 131072;
constexpr unsigned L_CC = L_P + 8192;
constexpr unsigned L_CT = L_CC + 4 * 272;
constexpr unsigned L_QP = L_CT;
constexpr unsigned L_FL = 159 * 1024;
constexpr unsigned F_LAND = L_FL + 0;
constexpr unsigned F_QP = L_FL + 16;
constexpr unsigned F_QC = L_FL + 32;
constexpr unsigned F_QS = L_FL + 48;
constexpr unsigned F_QR = L_FL + 64;
constexpr unsigned F_Q0 = L_FL + 80;
constexpr unsigned F_P = L_FL + 96;
constexpr unsigned F_O = L_FL + 112;
constexpr unsigned F_CC = L_FL + 128;
constexpr unsigned F_CT = L_FL + 144;
constexpr unsigned F_D = L_FL + 160;
constexpr unsigned F_FR = L_FL + 176;
constexpr unsigned L_MT = L_FL + 512;
constexpr unsigned L_WB = L_CT + 16 * 272;
constexpr unsigned L_FR = L_WB + 256;
constexpr unsigned L_LT = L_FR + 256;
constexpr unsigned LDS_BYTES = 163840;
static_assert(L_LT + 512 <= L_FL && L_MT + 512 <= LDS_BYTES && (L_MT % 16) == 0 && (L_CT % 16) == 0, "lds map");

constexpr int AUX_SC1 = 16;
constexpr unsigned SENT = 0xFFFFFFFFu;
constexpr float QSCALE = 0.08838834764831845f * 1.4426950408889634f;
#define THR 6
#define APRIO 1
#define TLBPF 0
#define WGATE 100000
#define CODEPF 0
#define BARPOS 0
#define QPLAIN 1
#define PACE 0
#define WOEARLY 1
#define THR2 12
#define THRSW 12
#define UVLAND 32u
#define QSLEEP 8
#define QFIRST 40
#define OSLEEP 4
#define C4POS 1
#define RSLEEP 2
constexpr unsigned SPIN_G = 1u << 16, SPIN_L = 1u << 21;

struct Params {
  const bf16_t* x[2]; const bf16_t* ckv[2]; const bf16_t* wq[2]; const bf16_t* wuk[2]; const bf16_t* wuv[2]; const bf16_t* wo[2];
  unsigned char* ws; bf16_t* out;
};
static_assert(sizeof(Params) == 112, "no padding");

#define RT(k) do { } while (0)

DI __amdgpu_buffer_rsrc_t mkrs(const void* p, unsigned bytes) { return __builtin_amdgcn_make_buffer_rsrc((void*)p, 0, (int)bytes, 0x00020000); }
DI v4u ldb(__amdgpu_buffer_rsrc_t r, unsigned voff, unsigned soff) { return __builtin_amdgcn_raw_buffer_load_b128(r, (int)voff, (int)soff, 0); }
DI f32x4 mfma16(v4u a, v4u b, f32x4 c) { return __builtin_amdgcn_mfma_f32_16x16x32_bf16(__builtin_bit_cast(bf16x8, a), __builtin_bit_cast(bf16x8, b), c, 0, 0, 0); }
DI f32x16 mfma32(v4u a, v4u b, f32x16 c) { return __builtin_amdgcn_mfma_f32_32x32x16_bf16(__builtin_bit_cast(bf16x8, a), __builtin_bit_cast(bf16x8, b), c, 0, 0, 0); }
DI unsigned pk(float lo, float hi) { f32x2 v = {lo, hi}; return __builtin_bit_cast(unsigned, __builtin_convertvector(v, bf16x2_t)); }
DI unsigned unsent(unsigned v) { return v == SENT ? 0x7FFF7FFFu : v; }
DI void cbar() { asm volatile("" ::: "memory"); }
DI void lgkm0() { asm volatile("s_waitcnt lgkmcnt(0)" ::: "memory"); }
DI void vm0() { asm volatile("s_waitcnt vmcnt(0)" ::: "memory"); }
DI v4u tfrag(const v4u* R, int i) {
  const unsigned sel = (i & 1) ? 0x07060302u : 0x05040100u; const int u = i >> 1;
  v4u f;
  f.x = __builtin_amdgcn_perm(R[1][u], R[0][u], sel); f.y = __builtin_amdgcn_perm(R[3][u], R[2][u], sel);
  f.z = __builtin_amdgcn_perm(R[5][u], R[4][u], sel); f.w = __builtin_amdgcn_perm(R[7][u], R[6][u], sel);
  return f;
}
DI void glds16(const void* gsrc, unsigned lds_dst) {
  unsigned keep;
  asm volatile("s_mov_b32 %0, m0\n\ts_mov_b32 m0, %2\n\ts_nop 0\n\tglobal_load_lds_dwordx4 %1, off\n\ts_mov_b32 m0, %0" : "=&s"(keep) : "v"(gsrc), "s"(lds_dst) : "memory");
}
DI v4u lds_ld(LAS unsigned char* L, unsigned off) { return *(LAS v4u*)(L + off); }
DI void lds_st(LAS unsigned char* L, unsigned off, v4u v) { *(LAS v4u*)(L + off) = v; }
DI void lsig(LAS unsigned char* L, unsigned off, unsigned val, int lane) {
  cbar();
  if (lane == 0) *(volatile LAS unsigned*)(L + off) = val;
  cbar();
}
DI bool lge4(LAS unsigned char* L, unsigned off, unsigned want) {
  const v4u f = *(volatile LAS v4u*)(L + off);
  const unsigned m = min(min(f.x, f.y), min(f.z, f.w));
  return (unsigned)__builtin_amdgcn_readfirstlane(m) >= want;
}
DI void lwait4(LAS unsigned char* L, unsigned off, unsigned want) {
  for (unsigned s = 0; s < SPIN_L; ++s) if (lge4(L, off, want)) break;
  cbar();
}
DI void lwait1(LAS unsigned char* L, unsigned off, unsigned want) {
  for (unsigned s = 0; s < SPIN_L; ++s) if ((unsigned)__builtin_amdgcn_readfirstlane(*(volatile LAS unsigned*)(L + off)) >= want) break;
  cbar();
}
template <int CTRL> DI float dpp(float x) { return __builtin_bit_cast(float, __builtin_amdgcn_mov_dpp(__builtin_bit_cast(int, x), CTRL, 0xf, 0xf, true)); }
DI float swap32_max(float x) { auto t = __builtin_amdgcn_permlane32_swap(__float_as_uint(x), __float_as_uint(x), false, false); return fmaxf(__uint_as_float(t[0]), __uint_as_float(t[1])); }
DI float swap16_max(float x) { auto t = __builtin_amdgcn_permlane16_swap(__float_as_uint(x), __float_as_uint(x), false, false); return fmaxf(__uint_as_float(t[0]), __uint_as_float(t[1])); }
DI float swap32_sum(float x) { auto t = __builtin_amdgcn_permlane32_swap(__float_as_uint(x), __float_as_uint(x), false, false); return __uint_as_float(t[0]) + __uint_as_float(t[1]); }
DI float swap16_sum(float x) { auto t = __builtin_amdgcn_permlane16_swap(__float_as_uint(x), __float_as_uint(x), false, false); return __uint_as_float(t[0]) + __uint_as_float(t[1]); }
DI void wait_cnt(unsigned char* p, unsigned want, int lane) {
  for (unsigned sp = 0; sp < SPIN_G; ++sp) {
    unsigned r = 0; const unsigned z = 0;
    if (lane == 0) asm volatile("global_atomic_add %0, %1, %2, off sc0 sc1\n\ts_waitcnt vmcnt(0)" : "=&v"(r) : "v"(p), "v"(z) : "memory");
    if ((unsigned)__builtin_amdgcn_readfirstlane(r) >= want) break;
    __builtin_amdgcn_s_sleep(RSLEEP);
  }
  cbar();
}
DI unsigned umax4(v4u v) { return max(max(v.x, v.y), max(v.z, v.w)); }

DI void pv_load(LAS unsigned char* L, int t, const unsigned (&tb)[8], s16x4 (&a)[16]) {
  const unsigned toff = (unsigned)t * 32768u;
#pragma unroll
  for (int tau = 0; tau < 4; ++tau)
#pragma unroll
    for (int s = 0; s < 2; ++s)
#pragma unroll
      for (int u = 0; u < 2; ++u)
        a[4 * tau + 2 * s + u] = __builtin_amdgcn_ds_read_tr16_b64_v4i16((LAS s16x4*)(L + (tb[2 * tau + u] + toff) + (8192u * u + 16384u * s)));
  __builtin_amdgcn_sched_barrier(0);
}
DI void pv_mma(const s16x4 (&a)[16], v4u p0, v4u p1, f32x16 (&o)[4]) {
#pragma unroll
  for (int tau = 0; tau < 4; ++tau) {
    o[tau] = mfma32(__builtin_bit_cast(v4u, __builtin_shufflevector(a[4 * tau], a[4 * tau + 1], 0, 1, 2, 3, 4, 5, 6, 7)), p0, o[tau]);
    o[tau] = mfma32(__builtin_bit_cast(v4u, __builtin_shufflevector(a[4 * tau + 2], a[4 * tau + 3], 0, 1, 2, 3, 4, 5, 6, 7)), p1, o[tau]);
  }
  __builtin_amdgcn_sched_barrier(0);
}
DI void pv012(LAS unsigned char* L, const unsigned (&tb)[8], int lane, f32x16 (&o)[4]) {
  s16x4 a0[16], a1[16];
  v4u pf[6];
#pragma unroll
  for (int i = 0; i < 6; ++i) pf[i] = lds_ld(L, L_P + i * 1024 + lane * 16);
  pv_load(L, 0, tb, a0);
  pv_load(L, 1, tb, a1);
  pv_mma(a0, pf[0], pf[1], o);
  pv_load(L, 2, tb, a0);
  pv_mma(a1, pf[2], pf[3], o);
  pv_mma(a0, pf[4], pf[5], o);
}
DI bool lge3(LAS unsigned char* L, unsigned off, unsigned want) {
  const v4u f = *(volatile LAS v4u*)(L + off);
  return (unsigned)__builtin_amdgcn_readfirstlane(min(min(f.x, f.y), f.z)) >= want;
}

__global__ void __launch_bounds__(512, 2) mla_fused(Params p) {
  extern __shared__ __attribute__((aligned(16))) unsigned char smem[];
  LAS unsigned char* L = (LAS unsigned char*)smem;
  const unsigned tg0 = (unsigned)__builtin_amdgcn_s_memrealtime();
#define PIN(us) do { while ((int)((unsigned)__builtin_amdgcn_s_memrealtime() - tg0) < (us) * 100) __builtin_amdgcn_s_sleep(1); } while (0)
  RT(0);
  const int tid = threadIdx.x, lane = tid & 63, w = __builtin_amdgcn_readfirstlane(tid >> 6);
  const int wg = blockIdx.x, xcd = wg & 7, idx = wg >> 3;
  const int br = xcd >> 2, b = xcd & 3, j = idx;
  const int h = (xcd & 3) * 8 + (idx >> 2), cs = idx & 3;
  const int rank = (xcd & 3) * 32 + idx;
  const int g = lane >> 4, n = lane & 15, hh = lane >> 5, r32 = lane & 31;
  const bf16_t* px = br ? p.x[1] : p.x[0]; const bf16_t* pckv = br ? p.ckv[1] : p.ckv[0]; const bf16_t* pwq = br ? p.wq[1] : p.wq[0];
  const bf16_t* pwuk = br ? p.wuk[1] : p.wuk[0]; const bf16_t* pwuv = br ? p.wuv[1] : p.wuv[0]; const bf16_t* pwo = br ? p.wo[1] : p.wo[0];
  unsigned char* wsb = p.ws + (size_t)br * WS_BR;
  bf16_t* pout = p.out;
  asm volatile("" :: "s"(px), "s"(pckv), "s"(pwq), "s"(pwuk), "s"(pwuv), "s"(pwo), "s"(wsb), "s"(pout));
  const __amdgpu_buffer_rsrc_t rs = mkrs(wsb, WS_BR);

  if (__builtin_expect(w < 4, 1)) {
    const int ww = w;
    __builtin_amdgcn_s_setprio(2);
    v4u xa[4], wqv[32], uk[8];
    const __amdgpu_buffer_rsrc_t rx = mkrs(px, 4096), rq = mkrs(pwq, 4u << 20), rk = mkrs(pwuk, 4u << 20);
    const unsigned vx = (n >> 2) * 1024 + ww * 64 + g * 16;
    const unsigned vq = (ww * 32 + g * 8) * 8192 + h * 256 + n * 16;
    const unsigned vk = (cs * 128 + ww * 32 + n) * 8192 + h * 256 + g * 16;
    const unsigned qo = WS_QA + ((n >> 2) * 32 + h) * 2048 + cs * 512 + ww * 128 + ((n & 1) * 4 + g) * 16;
#pragma unroll
    for (int i = 0; i < 8; ++i) wqv[i] = ldb(rq, vq, (((0 + cs) & 3) * 128 + i) * 8192);
    if ((n & 3) < 2) __builtin_amdgcn_raw_buffer_store_b128(v4u{0u, 0u, 0u, 0u}, rs, qo, 0, AUX_SC1);
    if (rank == 0 && lane < 32) __builtin_amdgcn_raw_buffer_store_b32(0u, rs, WS_C3 + hh * 0 + (lane >> 4) * (64 * 256) + (ww * 16 + n) * 256, 0, AUX_SC1);
#pragma unroll
    for (int q = 0; q < 4; ++q) xa[q] = ldb(rx, vx, ((q + cs) & 3) * 256);
#pragma unroll
    for (int i = 0; i < 4; ++i) uk[i] = ldb(rk, vk + i * 64, 0);
#pragma unroll
    for (int i = 0; i < 4; ++i) uk[4 + i] = ldb(rk, vk + i * 64, 16 * 8192);
#define BAR1(pos) do { if (BARPOS == (pos)) { __builtin_amdgcn_sched_barrier(0); __builtin_amdgcn_s_barrier(); __builtin_amdgcn_sched_barrier(0); } } while (0)
    __builtin_amdgcn_sched_barrier(0);
    BAR1(0);
    RT(1);
    u64 T;
    asm volatile("s_load_dwordx2 %0, %1, 0x0\n\ts_waitcnt lgkmcnt(0)" : "=s"(T) : "s"(wsb) : "memory");
    T += 2;
    f32x4 qa[8];
#pragma unroll
    for (int i = 0; i < 8; ++i) qa[i] = f32x4{0.f, 0.f, 0.f, 0.f};
    asm volatile("" : "+v"(wqv[7]));
    __builtin_amdgcn_sched_barrier(0);
    BAR1(1);
#pragma unroll
    for (int q = 1; q < 4; ++q)
#pragma unroll
      for (int i = 0; i < 8; ++i) wqv[8 * q + i] = ldb(rq, vq, (((q + cs) & 3) * 128 + i) * 8192);
    __builtin_amdgcn_sched_barrier(0);
    BAR1(2);
    RT(15);
    const unsigned Ta = (unsigned)T | 1u;
#pragma unroll
    for (int q = 0; q < 4; ++q)
#pragma unroll
      for (int i = 0; i < 8; ++i) qa[i] = mfma16(xa[q], tfrag(&wqv[8 * q], i), qa[i]);
    {
      v4u v; v.x = pk(qa[0][0], qa[1][0]); v.y = pk(qa[2][0], qa[3][0]); v.z = pk(qa[4][0], qa[5][0]); v.w = pk(qa[6][0], qa[7][0]);
      lds_st(L, L_QP + (4 * g + ww) * 272 + n * 16, v);
    }
    BAR1(3);
    lsig(L, F_QP + 4 * ww, 1u, lane);
    lwait4(L, F_QP, 1u);
    {
      v4u qb[4];
#pragma unroll
      for (int s = 0; s < 4; ++s) qb[s] = lds_ld(L, L_QP + n * 272 + s * 64 + g * 16);
      lsig(L, F_QC + 4 * ww, 1u, lane);
      f32x4 e0 = {0.f, 0.f, 0.f, 0.f}, e1 = {0.f, 0.f, 0.f, 0.f};
#pragma unroll
      for (int s = 0; s < 4; ++s) { e0 = mfma16(uk[s], qb[s], e0); e1 = mfma16(uk[4 + s], qb[s], e1); }
#pragma unroll
      for (int i = 0; i < 4; ++i) {
        e0[i] += dpp<0xB1>(e0[i]); e0[i] += dpp<0x4E>(e0[i]);
        e1[i] += dpp<0xB1>(e1[i]); e1[i] += dpp<0x4E>(e1[i]);
      }
      const int u = n & 3;
      const f32x4 e = (u & 1) ? e1 : e0;
      v4u o; o.x = pk(e[0] * QSCALE, e[1] * QSCALE); o.y = Ta; o.z = pk(e[2] * QSCALE, e[3] * QSCALE); o.w = Ta;
      vm0();
      if (u < 2) __builtin_amdgcn_raw_buffer_store_b128(o, rs, qo, 0, AUX_SC1);
    }
    RT(3);
    v4u qf[32];
    {
      v4u gq[16];
      const unsigned gv = WS_QA + (b * 32 + 8 * ww) * 2048 + lane * 16;
      PIN(15);
      cbar();
      RT(2);
#pragma unroll
      for (int i = 0; i < 16; ++i) gq[i] = __builtin_amdgcn_raw_buffer_load_b128(rs, gv + (i & 3) * 1024, (i >> 2) * 4096, 0);
      for (unsigned sp = 0; sp < SPIN_G; ++sp) {
        unsigned bad = 0;
#pragma unroll
        for (int i = 0; i < 16; ++i) bad |= (gq[i].y ^ Ta) | (gq[i].w ^ Ta);
        if (__all(bad == 0u)) break;
        __builtin_amdgcn_s_sleep(QSLEEP);
        cbar();
#pragma unroll
        for (int i = 0; i < 16; ++i) gq[i] = __builtin_amdgcn_raw_buffer_load_b128(rs, gv + (i & 3) * 1024, (i >> 2) * 4096, AUX_SC1);
      }
      cbar();
      RT(4);
      lwait4(L, F_QC, 1u);
      const unsigned sx = ((unsigned)(lane >> 2) & 3u) | (((unsigned)(lane >> 1) & 1u) << 2);
      const unsigned wb = L_STG + (unsigned)(lane >> 2) * 1024u + (((unsigned)lane >> 1) & 1u) * 512u + (lane & 1) * 8u + (unsigned)ww * 128u;
#pragma unroll
      for (int i = 0; i < 16; ++i) {
        v2u d; d.x = gq[i].x; d.y = gq[i].z;
        *(LAS v2u*)(L + wb + (i & 1) * 16384 + (((unsigned)(i >> 1) ^ sx) * 16u)) = d;
      }
    }
    lsig(L, F_QS + 4 * ww, 1u, lane);
    const unsigned fr = ((r32 & 3) << 2) | ((r32 >> 2) & 3);
    const unsigned rowb = 1024u * r32 + 16u * ((unsigned)hh ^ fr);
    unsigned ab[8], qb4[4];
#pragma unroll
    for (int s = 0; s < 8; ++s) { ab[s] = (L_C + 32768u * ww + rowb) ^ (32u * s); asm volatile("" : "+v"(ab[s])); }
#pragma unroll
    for (int s = 0; s < 4; ++s) { qb4[s] = L_STG + 512u * hh + (((unsigned)r32 ^ ((unsigned)s | ((unsigned)hh << 2))) * 16u); asm volatile("" : "+v"(qb4[s])); }
    lwait4(L, F_QS, 1u);
    f32x16 sp;
#pragma unroll
    for (int i = 0; i < 16; ++i) sp[i] = 0.f;
    if (lge4(L, F_LAND, 8u * (ww + 1))) {
      cbar();
      RT(7);
      v4u af[32];
#pragma unroll
      for (int s = 0; s < 6; ++s) { qf[s] = lds_ld(L, qb4[s & 3] + 1024u * s); af[s] = lds_ld(L, ab[s]); }
      __builtin_amdgcn_sched_barrier(0);
#pragma unroll
      for (int s = 0; s < 32; ++s) {
        if (s + 6 < 32) { qf[s + 6] = lds_ld(L, qb4[(s + 6) & 3] + 1024u * (s + 6)); af[s + 6] = lds_ld(L, ab[(s + 6) & 7] + 256u * ((s + 6) >> 3)); }
        sp = mfma32(af[s], qf[s], sp);
        if (s == 0) lsig(L, F_Q0 + 4 * ww, 1u, lane);
        __builtin_amdgcn_sched_barrier(0);
      }
      lsig(L, F_QR + 4 * ww, 1u, lane);
    } else {
      qf[0] = lds_ld(L, qb4[0]);
      lsig(L, F_Q0 + 4 * ww, 1u, lane);
#pragma unroll
      for (int i = 1; i < 32; ++i) qf[i] = lds_ld(L, qb4[i & 3] + 1024u * i);
      lgkm0();
      lsig(L, F_QR + 4 * ww, 1u, lane);
      lwait4(L, F_LAND, 8u * (ww + 1));
      RT(7);
      v4u af[32];
#pragma unroll
      for (int s = 0; s < 8; ++s) af[s] = lds_ld(L, ab[s]);
      __builtin_amdgcn_sched_barrier(0);
#pragma unroll
      for (int s = 0; s < 32; ++s) {
        if (s + 8 < 32) af[s + 8] = lds_ld(L, ab[s & 7] + 256u * ((s + 8) >> 3));
        sp = mfma32(af[s], qf[s], sp);
        __builtin_amdgcn_sched_barrier(0);
      }
    }
    {
      float mx = fmaxf(fmaxf(sp[0], sp[1]), fmaxf(sp[2], sp[3]));
#pragma unroll
      for (int i = 4; i < 16; i += 2) mx = fmaxf(mx, fmaxf(sp[i], sp[i + 1]));
      float mrun = __builtin_ceilf(swap32_max(mx));
      { const unsigned mb = __float_as_uint(mrun); if (lane < 32) *(volatile LAS unsigned*)(L + L_MT + lane * 16 + ww * 4) = mb == SENT ? 0x7FC00000u : mb; }
      v4u mt;
      for (unsigned s = 0; s < SPIN_L; ++s) {
        mt = *(volatile LAS v4u*)(L + L_MT + r32 * 16);
        if (__all(max(max(mt.x, mt.y), mt.z) != SENT)) break;
      }
      mrun = fmaxf(fmaxf(mrun, __uint_as_float(mt.x)), fmaxf(__uint_as_float(mt.y), __uint_as_float(mt.z)));
      float pe[16];
#pragma unroll
      for (int i = 0; i < 16; ++i) pe[i] = __builtin_amdgcn_exp2f(sp[i] - mrun);
      v4u p0, p1;
      p0.x = pk(pe[0], pe[1]); p0.y = pk(pe[2], pe[3]); p0.z = pk(pe[4], pe[5]); p0.w = pk(pe[6], pe[7]);
      p1.x = pk(pe[8], pe[9]); p1.y = pk(pe[10], pe[11]); p1.z = pk(pe[12], pe[13]); p1.w = pk(pe[14], pe[15]);
      lwait4(L, F_QR, 1u);
      lds_st(L, L_P + ww * 2048 + lane * 16, p0); lds_st(L, L_P + ww * 2048 + 1024 + lane * 16, p1);
      lsig(L, F_P + 4 * ww, 1u, lane);
      float ls = (((pe[0] + pe[1]) + (pe[2] + pe[3])) + ((pe[4] + pe[5]) + (pe[6] + pe[7]))) + (((pe[8] + pe[9]) + (pe[10] + pe[11])) + ((pe[12] + pe[13]) + (pe[14] + pe[15])));
      ls = swap32_sum(ls);
      if (lane < 32) *(LAS float*)(L + L_LT + lane * 16 + ww * 4) = ls;
      lsig(L, F_P + 4 * ww, 2u, lane);
    }
    RT(8);
    wait_cnt(wsb + WS_C3 + ((cs * 4 + ww) * 4 + (h & 3)) * 256, 32u, lane);
    RT(11);
    {
      const v2u rc = __builtin_amdgcn_raw_buffer_load_b64(rs, WS_ML + ((ww * 32 + r32) * 4 + cs) * 256 + h * 8, 0, AUX_SC1);
      v4u ov[8];
      const unsigned oo = WS_OP + ((ww * 32 + g * 8) * 4 + cs) * 8192 + h * 256 + n * 16;
#pragma unroll
      for (int i = 0; i < 8; ++i) ov[i] = __builtin_amdgcn_raw_buffer_load_b128(rs, oo, i * 32768, AUX_SC1);
      __builtin_amdgcn_sched_barrier(0);
      const float mj = __uint_as_float(rc.x), lj = __uint_as_float(rc.y);
      float M = mj;
      M = fmaxf(M, dpp<0xB1>(M)); M = fmaxf(M, dpp<0x4E>(M)); M = fmaxf(M, dpp<0x124>(M)); M = fmaxf(M, dpp<0x128>(M));
      M = swap16_max(M);
      const float ej = __builtin_amdgcn_exp2f(mj - M);
      float Ls = ej * lj;
      Ls += dpp<0xB1>(Ls); Ls += dpp<0x4E>(Ls); Ls += dpp<0x124>(Ls); Ls += dpp<0x128>(Ls);
      Ls = swap16_sum(Ls);
      const float inv = 1.0f / Ls;
      if (lane < 32) *(LAS unsigned short*)(L + L_WB + ww * 64 + lane * 2) = (unsigned short)(__float_as_uint(ej) >> 16);
      cbar();
      const v4u wf = *(volatile LAS v4u*)(L + L_WB + ww * 64 + g * 16);
      float c8[8];
#pragma unroll
      for (int i = 0; i < 8; ++i) { const f32x4 d = mfma16(wf, tfrag(ov, i), f32x4{0.f, 0.f, 0.f, 0.f}); c8[i] = d[0] * inv; }
      v4u v; v.x = pk(c8[0], c8[1]); v.y = pk(c8[2], c8[3]); v.z = pk(c8[4], c8[5]); v.w = pk(c8[6], c8[7]);
      if (lane < 16) lds_st(L, L_CC + ww * 272 + n * 16, v);
    }
    lsig(L, F_CC + 4 * ww, 1u, lane);
    RT(12);
    if (ww == 0) {
      const int u2 = h & 1, bo = (h >> 1) & 3, a4 = h >> 3;
      const unsigned so = WS_OUTP + (lane >> 2) * 8192 + cs * 2048 + u2 * 1024 + (bo * 16 + a4 * 4 + (lane & 3)) * 16;
      {
        unsigned char* cp = wsb + WS_C4 + ((cs * 4 + (lane & 3)) * 4 + (h & 3)) * 256;
        for (unsigned sp = 0; sp < SPIN_G; ++sp) {
          unsigned r = 32u; const unsigned z = 0;
          if (lane < 4) asm volatile("global_atomic_add %0, %1, %2, off sc0 sc1\n\ts_waitcnt vmcnt(0)" : "=&v"(r) : "v"(cp), "v"(z) : "memory");
          if (__all(r >= 32u)) break;
          __builtin_amdgcn_s_sleep(RSLEEP);
        }
        cbar();
      }
      RT(13);
      v4u pv[8];
      for (unsigned sp = 0; sp < SPIN_G; ++sp) {
        cbar();
#pragma unroll
        for (int i = 0; i < 8; ++i) pv[i] = __builtin_amdgcn_raw_buffer_load_b128(rs, so, i * (16 * 8192), AUX_SC1);
        unsigned m = 0;
#pragma unroll
        for (int i = 0; i < 8; ++i) m = max(m, umax4(pv[i]));
        if (__all(m != SENT)) break;
        __builtin_amdgcn_s_sleep(OSLEEP);
      }
      cbar();
      RT(5);
      f32x4 s = ((__builtin_bit_cast(f32x4, pv[0]) + __builtin_bit_cast(f32x4, pv[1])) + (__builtin_bit_cast(f32x4, pv[2]) + __builtin_bit_cast(f32x4, pv[3]))) +
                ((__builtin_bit_cast(f32x4, pv[4]) + __builtin_bit_cast(f32x4, pv[5])) + (__builtin_bit_cast(f32x4, pv[6]) + __builtin_bit_cast(f32x4, pv[7])));
#pragma unroll
      for (int i = 0; i < 4; ++i) {
        s[i] += dpp<0x124>(s[i]); s[i] += dpp<0x128>(s[i]);
        s[i] = swap32_sum(swap16_sum(s[i]));
      }
      if (lane < 4) {
        v2u o2; o2.x = pk(s[0], s[1]); o2.y = pk(s[2], s[3]);
        *(GAS v2u*)(pout + bo * 1024 + br * 512 + cs * 128 + (a4 * 4 + lane) * 8 + u2 * 4) = o2;
      }
      if (rank == 0 && lane == 0) __hip_atomic_store((GAS u64*)(wsb + WS_EPOCH), T, __ATOMIC_RELAXED, __HIP_MEMORY_SCOPE_AGENT);
      RT(6);
    }
  } else {
    const int aw = w - 4;
    const unsigned tk0 = (unsigned)__builtin_amdgcn_s_memrealtime();
    if (aw == 1) {
      if (lane < 48) *(volatile LAS unsigned*)(L + L_FL + lane * 4) = 0u;
      *(volatile LAS v2u*)(L + L_MT + lane * 8) = v2u{SENT, SENT};
    }
    lgkm0();
    RT(1);
    __builtin_amdgcn_s_barrier();
    RT(2);
    v4u r127 = {0u, 0u, 0u, 0u};
    {
      const bf16_t* C = pckv + ((size_t)b * 4096 + j * 128) * 512;
#define DMA_ROW(r4_, k_) glds16(C + (size_t)(16 * (r4_) + 4 * (k_) + aw) * 512 + (unsigned)(lane ^ (((unsigned)aw << 2) | (unsigned)(k_))) * 8, \
                                (unsigned)(size_t)smem + L_C + (16 * (r4_) + 4 * (k_) + aw) * 1024)
#pragma unroll
      for (int i = 0; i < 32; ++i) {
        if (i == 31 && aw == 3) r127 = *(const GAS v4u*)(C + (size_t)127 * 512 + (unsigned)(lane ^ 15) * 8);
        else DMA_ROW(i >> 2, i & 3);
        const int win = i < THRSW ? THR : THR2, pwin = (i - 1) < THRSW ? THR : THR2;
        if (win == THR) asm volatile("s_waitcnt vmcnt(%0)" :: "n"(THR) : "memory"); else asm volatile("s_waitcnt vmcnt(%0)" :: "n"(THR2) : "memory");
        int done = i + 1 - win, best = 0;
        for (int k = 0; k < i; ++k) { const int d = k + 1 - (k < THRSW ? THR : THR2); if (d > best) best = d; }
        (void)pwin;
        if (done > 0 && (done >> 3) > (best > 0 ? best >> 3 : 0)) { if (lane == 0) *(volatile LAS unsigned*)(L + F_LAND + 4 * aw) = (unsigned)(done & ~7); }
      }
      if (32 - THR2 < 24) {
        asm volatile("s_waitcnt vmcnt(8)" ::: "memory");
        if (lane == 0) *(volatile LAS unsigned*)(L + F_LAND + 4 * aw) = 24u;
      }
    }
    RT(3);
    unsigned tb[8];
    {
      const unsigned blk = (lane >> 4) & 1, q = (lane & 15) >> 2, pp = lane & 3;
      const unsigned b0 = L_C + 1024u * (4u * hh + q) + 256u * aw + 64u * q + 16u * ((2u * blk + (pp >> 1)) ^ (unsigned)hh) + 8u * (pp & 1);
#pragma unroll
      for (int tau = 0; tau < 4; ++tau) { tb[2 * tau] = b0 ^ (64u * tau); tb[2 * tau + 1] = b0 ^ (64u * tau) ^ 32u; }
    }
    f32x16 o[4];
#pragma unroll
    for (int tau = 0; tau < 4; ++tau)
#pragma unroll
      for (int i = 0; i < 16; ++i) o[tau][i] = 0.f;
    __builtin_amdgcn_s_setprio(APRIO);
    const bool early = lge3(L, F_P, 1u);
    if (early) { cbar(); pv012(L, tb, lane, o); }
    RT(4);
    vm0();
    if (aw == 3) { lwait4(L, F_Q0, 1u); lds_st(L, L_C + 127 * 1024 + lane * 16, r127); cbar(); }
    if (lane == 0) *(volatile LAS unsigned*)(L + F_LAND + 4 * aw) = 32u;
    RT(5);
    v4u uv[8], wov[32];
    const __amdgpu_buffer_rsrc_t rv = mkrs(pwuv, 4u << 20), ro = mkrs(pwo, 4u << 20);
    const unsigned vv = (cs * 128 + aw * 32 + g * 8) * 8192 + h * 256 + n * 16;
    const unsigned vo = (h * 128 + g * 8) * 1024 + aw * 256 + n * 16;
    const bool wnow = !early && (int)((unsigned)__builtin_amdgcn_s_memrealtime() - tk0) < WGATE;
    if (wnow) {
#pragma unroll
      for (int i = 0; i < 8; ++i) uv[i] = ldb(rv, vv, i * 8192);
#pragma unroll
      for (int i = 0; i < 8; ++i) wov[i] = ldb(ro, vo, (cs * 32 + i) * 1024);
    }
    __builtin_amdgcn_sched_barrier(0);
    if (!early) {
      for (unsigned s = 0; s < SPIN_L; ++s) if (lge3(L, F_P, 1u)) break;
      cbar();
      pv012(L, tb, lane, o);
    }
    {
      s16x4 pa[16];
      lwait4(L, F_LAND, 32u);
      pv_load(L, 3, tb, pa);
      RT(6);
      lwait1(L, F_P + 12, 1u);
      RT(7);
      const f32x4 mt = *(LAS f32x4*)(L + L_MT + r32 * 16);
      const v4u p0 = lds_ld(L, L_P + 6144 + lane * 16), p1 = lds_ld(L, L_P + 7168 + lane * 16);
      const float m012 = fmaxf(fmaxf(mt[0], mt[1]), mt[2]), mf = fmaxf(m012, mt[3]);
      const float al = __builtin_amdgcn_exp2f(m012 - mf);
#pragma unroll
      for (int tau = 0; tau < 4; ++tau) {
        o[tau] *= al;
        o[tau] = mfma32(__builtin_bit_cast(v4u, __builtin_shufflevector(pa[4 * tau], pa[4 * tau + 1], 0, 1, 2, 3, 4, 5, 6, 7)), p0, o[tau]);
        o[tau] = mfma32(__builtin_bit_cast(v4u, __builtin_shufflevector(pa[4 * tau + 2], pa[4 * tau + 3], 0, 1, 2, 3, 4, 5, 6, 7)), p1, o[tau]);
      }
#pragma unroll
      for (int tau = 0; tau < 4; ++tau)
#pragma unroll
        for (int v = 0; v < 2; ++v) {
          const unsigned ax = pk(o[tau][8 * v + 0], o[tau][8 * v + 1]), ay = pk(o[tau][8 * v + 2], o[tau][8 * v + 3]);
          const unsigned bx = pk(o[tau][8 * v + 4], o[tau][8 * v + 5]), by = pk(o[tau][8 * v + 6], o[tau][8 * v + 7]);
          auto sx = __builtin_amdgcn_permlane32_swap(ax, bx, false, false);
          auto sy = __builtin_amdgcn_permlane32_swap(ay, by, false, false);
          v4u val; val.x = sx[0]; val.y = sy[0]; val.z = sx[1]; val.w = sy[1];
          const unsigned ch = 4u * tau + 2u * v + (unsigned)hh;
          lds_st(L, L_C + 1024u * r32 + 256u * aw + 16u * (ch ^ (unsigned)(r32 & 7)), val);
        }
      cbar();
      RT(8);
      v4u ov[8];
#pragma unroll
      for (int i = 0; i < 8; ++i) { const int hr = 4 * i + g; ov[i] = lds_ld(L, L_C + 1024u * hr + 256u * aw + 16u * (unsigned)(n ^ (hr & 7))); }
#pragma unroll
      for (int i = 0; i < 8; ++i) __builtin_amdgcn_raw_buffer_store_b128(ov[i], rs, WS_OP + ((b * 32 + j) * 4 + aw) * 8192 + lane * 16 + (i & 3) * 1024, (i >> 2) * 4096, AUX_SC1);
      lwait4(L, F_P, 2u);
      const f32x4 lt = *(LAS f32x4*)(L + L_LT + r32 * 16);
      v2u ml; ml.x = __float_as_uint(mf); ml.y = __float_as_uint(((lt[0] + lt[1]) + lt[2]) * al + lt[3]);
      if (lane < 32) __builtin_amdgcn_raw_buffer_store_b64(ml, rs, WS_ML + ((b * 32 + j) * 4 + aw) * 256 + lane * 8, 0, AUX_SC1);
    }
    RT(9);
    vm0();
    RT(10);
    if (lane < 4) __hip_atomic_fetch_add((GAS unsigned*)(wsb + WS_C3 + ((aw * 4 + b) * 4 + lane) * 256), 1u, __ATOMIC_RELAXED, __HIP_MEMORY_SCOPE_AGENT);
    if (!wnow) {
#pragma unroll
      for (int i = 0; i < 8; ++i) uv[i] = ldb(rv, vv, i * 8192);
#pragma unroll
      for (int i = 0; i < 8; ++i) wov[i] = ldb(ro, vo, (cs * 32 + i) * 1024);
    }
    {
      const v4u sv = {SENT, SENT, SENT, SENT};
      __builtin_amdgcn_raw_buffer_store_b128(sv, rs, WS_OUTP + rank * 8192 + aw * 2048 + lane * 16, 0, AUX_SC1);
      __builtin_amdgcn_raw_buffer_store_b128(sv, rs, WS_OUTP + rank * 8192 + aw * 2048 + 1024 + lane * 16, 0, AUX_SC1);
    }
#pragma unroll
    for (int q = 1; q < 4; ++q)
#pragma unroll
      for (int i = 0; i < 8; ++i) wov[8 * q + i] = ldb(ro, vo, (((q + cs) & 3) * 32 + i) * 1024);
    __builtin_amdgcn_sched_barrier(0);
    v4u uvf[8], wf[32];
#pragma unroll
    for (int i = 0; i < 8; ++i) uvf[i] = tfrag(uv, i);
#pragma unroll
    for (int q = 0; q < 4; ++q)
#pragma unroll
      for (int i = 0; i < 8; ++i) wf[8 * q + i] = tfrag(&wov[8 * q], i);
#pragma unroll
    for (int i = 0; i < 8; ++i) asm volatile("" : "+v"(uvf[i]));
#pragma unroll
    for (int i = 0; i < 32; ++i) asm volatile("" : "+v"(wf[i]));
    vm0();
    RT(11);
    lwait4(L, F_CC, 1u);
    RT(12);
    {
      const v4u af = lds_ld(L, L_CC + (n >> 2) * 272 + aw * 64 + g * 16);
      float c8[8];
#pragma unroll
      for (int i = 0; i < 8; ++i) { const f32x4 d = mfma16(af, uvf[i], f32x4{0.f, 0.f, 0.f, 0.f}); c8[i] = d[0]; }
      v4u v; v.x = pk(c8[0], c8[1]); v.y = pk(c8[2], c8[3]); v.z = pk(c8[4], c8[5]); v.w = pk(c8[6], c8[7]);
      lds_st(L, L_CT + (4 * g + aw) * 272 + n * 16, v);
    }
    lsig(L, F_CT + 4 * aw, 1u, lane);
    lwait4(L, F_CT, 1u);
    RT(14);
#define C4ADD() do { if (lane < 4) __hip_atomic_fetch_add((GAS unsigned*)(wsb + WS_C4 + ((aw * 4 + (rank >> 5)) * 4 + lane) * 256), 1u, __ATOMIC_RELAXED, __HIP_MEMORY_SCOPE_AGENT); } while (0)
    {
      v4u af[4];
#pragma unroll
      for (int q = 0; q < 4; ++q) af[q] = lds_ld(L, L_CT + n * 272 + ((q + cs) & 3) * 64 + g * 16);
      f32x4 d[8];
#pragma unroll
      for (int i = 0; i < 8; ++i) d[i] = f32x4{0.f, 0.f, 0.f, 0.f};
#pragma unroll
      for (int q = 0; q < 4; ++q)
#pragma unroll
        for (int i = 0; i < 8; ++i) d[i] = mfma16(af[q], wf[8 * q + i], d[i]);
      __builtin_amdgcn_sched_barrier(0);
      C4ADD();
      __builtin_amdgcn_sched_barrier(0);
      v4u s0, s1;
      unsigned sv[8];
#pragma unroll
      for (int i = 0; i < 8; ++i) { const unsigned x = __float_as_uint((d[i][0] + d[i][1]) + (d[i][2] + d[i][3])); sv[i] = x == SENT ? 0x7FC00000u : x; }
      s0.x = sv[0]; s0.y = sv[1]; s0.z = sv[2]; s0.w = sv[3]; s1.x = sv[4]; s1.y = sv[5]; s1.z = sv[6]; s1.w = sv[7];
      __builtin_amdgcn_raw_buffer_store_b128(s0, rs, WS_OUTP + rank * 8192 + aw * 2048 + lane * 16, 0, AUX_SC1);
      __builtin_amdgcn_raw_buffer_store_b128(s1, rs, WS_OUTP + rank * 8192 + aw * 2048 + 1024 + lane * 16, 0, AUX_SC1);
    }
    RT(13);
  }
}
}

extern "C" void kernel_launch(void* const* d_in, const int* in_sizes, int n_in, void* d_out, int out_size, void* d_ws, size_t ws_size,
                              hipStream_t stream) {
  (void)in_sizes; (void)n_in; (void)out_size; (void)ws_size;
  mk::Params p;
  for (int br = 0; br < 2; ++br) {
    p.x[br] = (const bf16_t*)d_in[6 * br + 0]; p.ckv[br] = (const bf16_t*)d_in[6 * br + 1]; p.wq[br] = (const bf16_t*)d_in[6 * br + 2];
    p.wuk[br] = (const bf16_t*)d_in[6 * br + 3]; p.wuv[br] = (const bf16_t*)d_in[6 * br + 4]; p.wo[br] = (const bf16_t*)d_in[6 * br + 5];
  }
  p.ws = (unsigned char*)d_ws; p.out = (bf16_t*)d_out;
  (void)hipFuncSetAttribute((const void*)mk::mla_fused, hipFuncAttributeMaxDynamicSharedMemorySize, (int)mk::LDS_BYTES);
  mk::mla_fused<<<256, 512, mk::LDS_BYTES, stream>>>(p);
}
```

```cpp
#include <hip/hip_runtime.h>
#include <stdint.h>

#define GAS __attribute__((address_space(1)))
#define LAS __attribute__((address_space(3)))
#define DI __device__ __forceinline__
typedef unsigned short bf16_t;
typedef unsigned v4u __attribute__((ext_vector_type(4)));
typedef unsigned v2u __attribute__((ext_vector_type(2)));
typedef float f32x2 __attribute__((ext_vector_type(2)));
typedef float f32x4 __attribute__((ext_vector_type(4)));
typedef float f32x16 __attribute__((ext_vector_type(16)));
typedef short bf16x8 __attribute__((ext_vector_type(8)));
typedef short s16x4 __attribute__((ext_vector_type(4)));
typedef __bf16 bf16x2_t __attribute__((ext_vector_type(2)));
typedef unsigned long long u64;

namespace mk {

constexpr unsigned WS_EPOCH = 0;
constexpr unsigned WS_C3 = 4096;
constexpr unsigned WS_C4 = WS_C3 + 64 * 256;
constexpr unsigned WS_ML = WS_C4 + 64 * 256;
constexpr unsigned WS_QA = WS_ML + 4 * 32 * 4 * 256;
constexpr unsigned WS_OUTP = WS_QA + 4 * 32 * 2048;
constexpr unsigned WS_OP = WS_OUTP + 128 * 8192;
constexpr unsigned WS_BR = 8u << 20;
static_assert(WS_OP + 32 * 4 * 4 * 32 * 256 <= WS_BR, "ws map");
constexpr unsigned WS_TOTAL = 2 * WS_BR;

constexpr unsigned L_C = 0;
constexpr unsigned L_STG = 127 * 1024;
constexpr unsigned L_P = 131072;
constexpr unsigned L_CC = L_P + 8192;
constexpr unsigned L_CT = L_CC + 4 * 272;
constexpr unsigned L_QP = L_CT;
constexpr unsigned L_FL = 159 * 1024;
constexpr unsigned F_LAND = L_FL + 0;
constexpr unsigned F_QP = L_FL + 16;
constexpr unsigned F_QC = L_FL + 32;
constexpr unsigned F_QS = L_FL + 48;
constexpr unsigned F_QR = L_FL + 64;
constexpr unsigned F_Q0 = L_FL + 80;
constexpr unsigned F_P = L_FL + 96;
constexpr unsigned F_O = L_FL + 112;
constexpr unsigned F_CC = L_FL + 128;
constexpr unsigned F_CT = L_FL + 144;
constexpr unsigned F_D = L_FL + 160;
constexpr unsigned F_FR = L_FL + 176;
constexpr unsigned L_MT = L_FL + 512;
constexpr unsigned L_WB = L_CT + 16 * 272;
constexpr unsigned L_FR = L_WB + 256;
constexpr unsigned L_LT = L_FR + 256;
constexpr unsigned LDS_BYTES = 163840;
static_assert(L_LT + 512 <= L_FL && L_MT + 512 <= LDS_BYTES && (L_MT % 16) == 0 && (L_CT % 16) == 0, "lds map");

constexpr int AUX_SC1 = 16;
constexpr unsigned SENT = 0xFFFFFFFFu;
constexpr float QSCALE = 0.08838834764831845f * 1.4426950408889634f;
#define THR 8
#define APRIO 1
#define TLBPF 0
#define WGATE 100000
#define CODEPF 0
#define BARPOS 0
#define QPLAIN 0
#define PACE 0
#define WOEARLY 1
#define THR2 16
#define THRSW 12
#define UVLAND 32u
#define QSLEEP 8
#define QFIRST 40
#define OSLEEP 4
#define C4POS 0
#define RSLEEP 2
constexpr unsigned SPIN_G = 1u << 16, SPIN_L = 1u << 21;

struct Params {
  const bf16_t* x[2]; const bf16_t* ckv[2]; const bf16_t* wq[2]; const bf16_t* wuk[2]; const bf16_t* wuv[2]; const bf16_t* wo[2];
  unsigned char* ws; bf16_t* out;
};
static_assert(sizeof(Params) == 112, "no padding");

#define RT(k) do { } while (0)

DI __amdgpu_buffer_rsrc_t mkrs(const void* p, unsigned bytes) { return __builtin_amdgcn_make_buffer_rsrc((void*)p, 0, (int)bytes, 0x00020000); }
DI v4u ldb(__amdgpu_buffer_rsrc_t r, unsigned voff, unsigned soff) { return __builtin_amdgcn_raw_buffer_load_b128(r, (int)voff, (int)soff, 0); }
DI f32x4 mfma16(v4u a, v4u b, f32x4 c) { return __builtin_amdgcn_mfma_f32_16x16x32_bf16(__builtin_bit_cast(bf16x8, a), __builtin_bit_cast(bf16x8, b), c, 0, 0, 0); }
DI f32x16 mfma32(v4u a, v4u b, f32x16 c) { return __builtin_amdgcn_mfma_f32_32x32x16_bf16(__builtin_bit_cast(bf16x8, a), __builtin_bit_cast(bf16x8, b), c, 0, 0, 0); }
DI unsigned pk(float lo, float hi) { f32x2 v = {lo, hi}; return __builtin_bit_cast(unsigned, __builtin_convertvector(v, bf16x2_t)); }
DI unsigned unsent(unsigned v) { return v == SENT ? 0x7FFF7FFFu : v; }
DI void cbar() { asm volatile("" ::: "memory"); }
DI void lgkm0() { asm volatile("s_waitcnt lgkmcnt(0)" ::: "memory"); }
DI void vm0() { asm volatile("s_waitcnt vmcnt(0)" ::: "memory"); }
DI v4u tfrag(const v4u* R, int i) {
  const unsigned sel = (i & 1) ? 0x07060302u : 0x05040100u; const int u = i >> 1;
  v4u f;
  f.x = __builtin_amdgcn_perm(R[1][u], R[0][u], sel); f.y = __builtin_amdgcn_perm(R[3][u], R[2][u], sel);
  f.z = __builtin_amdgcn_perm(R[5][u], R[4][u], sel); f.w = __builtin_amdgcn_perm(R[7][u], R[6][u], sel);
  return f;
}
DI void glds16(const void* gsrc, unsigned lds_dst) {
  unsigned keep;
  asm volatile("s_mov_b32 %0, m0\n\ts_mov_b32 m0, %2\n\ts_nop 0\n\tglobal_load_lds_dwordx4 %1, off\n\ts_mov_b32 m0, %0" : "=&s"(keep) : "v"(gsrc), "s"(lds_dst) : "memory");
}
DI v4u lds_ld(LAS unsigned char* L, unsigned off) { return *(LAS v4u*)(L + off); }
DI void lds_st(LAS unsigned char* L, unsigned off, v4u v) { *(LAS v4u*)(L + off) = v; }
DI void lsig(LAS unsigned char* L, unsigned off, unsigned val, int lane) {
  cbar();
  if (lane == 0) *(volatile LAS unsigned*)(L + off) = val;
  cbar();
}
DI bool lge4(LAS unsigned char* L, unsigned off, unsigned want) {
  const v4u f = *(volatile LAS v4u*)(L + off);
  const unsigned m = min(min(f.x, f.y), min(f.z, f.w));
  return (unsigned)__builtin_amdgcn_readfirstlane(m) >= want;
}
DI void lwait4(LAS unsigned char* L, unsigned off, unsigned want) {
  for (unsigned s = 0; s < SPIN_L; ++s) if (lge4(L, off, want)) break;
  cbar();
}
DI void lwait1(LAS unsigned char* L, unsigned off, unsigned want) {
  for (unsigned s = 0; s < SPIN_L; ++s) if ((unsigned)__builtin_amdgcn_readfirstlane(*(volatile LAS unsigned*)(L + off)) >= want) break;
  cbar();
}
template <int CTRL> DI float dpp(float x) { return __builtin_bit_cast(float, __builtin_amdgcn_mov_dpp(__builtin_bit_cast(int, x), CTRL, 0xf, 0xf, true)); }
DI float swap32_max(float x) { auto t = __builtin_amdgcn_permlane32_swap(__float_as_uint(x), __float_as_uint(x), false, false); return fmaxf(__uint_as_float(t[0]), __uint_as_float(t[1])); }
DI float swap16_max(float x) { auto t = __builtin_amdgcn_permlane16_swap(__float_as_uint(x), __float_as_uint(x), false, false); return fmaxf(__uint_as_float(t[0]), __uint_as_float(t[1])); }
DI float swap32_sum(float x) { auto t = __builtin_amdgcn_permlane32_swap(__float_as_uint(x), __float_as_uint(x), false, false); return __uint_as_float(t[0]) + __uint_as_float(t[1]); }
DI float swap16_sum(float x) { auto t = __builtin_amdgcn_permlane16_swap(__float_as_uint(x), __float_as_uint(x), false, false); return __uint_as_float(t[0]) + __uint_as_float(t[1]); }
DI void wait_cnt(unsigned char* p, unsigned want, int lane) {
  for (unsigned sp = 0; sp < SPIN_G; ++sp) {
    unsigned r = 0; const unsigned z = 0;
    if (lane == 0) asm volatile("global_atomic_add %0, %1, %2, off sc0 sc1\n\ts_waitcnt vmcnt(0)" : "=&v"(r) : "v"(p), "v"(z) : "memory");
    if ((unsigned)__builtin_amdgcn_readfirstlane(r) >= want) break;
    __builtin_amdgcn_s_sleep(RSLEEP);
  }
  cbar();
}
DI unsigned umax4(v4u v) { return max(max(v.x, v.y), max(v.z, v.w)); }

DI void pv_load(LAS unsigned char* L, int t, const unsigned (&tb)[8], s16x4 (&a)[16]) {
  const unsigned toff = (unsigned)t * 32768u;
#pragma unroll
  for (int tau = 0; tau < 4; ++tau)
#pragma unroll
    for (int s = 0; s < 2; ++s)
#pragma unroll
      for (int u = 0; u < 2; ++u)
        a[4 * tau + 2 * s + u] = __builtin_amdgcn_ds_read_tr16_b64_v4i16((LAS s16x4*)(L + (tb[2 * tau + u] + toff) + (8192u * u + 16384u * s)));
  __builtin_amdgcn_sched_barrier(0);
}
DI void pv_mma(const s16x4 (&a)[16], v4u p0, v4u p1, f32x16 (&o)[4]) {
#pragma unroll
  for (int tau = 0; tau < 4; ++tau) {
    o[tau] = mfma32(__builtin_bit_cast(v4u, __builtin_shufflevector(a[4 * tau], a[4 * tau + 1], 0, 1, 2, 3, 4, 5, 6, 7)), p0, o[tau]);
    o[tau] = mfma32(__builtin_bit_cast(v4u, __builtin_shufflevector(a[4 * tau + 2], a[4 * tau + 3], 0, 1, 2, 3, 4, 5, 6, 7)), p1, o[tau]);
  }
  __builtin_amdgcn_sched_barrier(0);
}
DI void pv012(LAS unsigned char* L, const unsigned (&tb)[8], int lane, s16x4 (&a0)[16], s16x4 (&a1)[16], f32x16 (&o)[4]) {
  v4u pf[6];
#pragma unroll
  for (int i = 0; i < 6; ++i) pf[i] = lds_ld(L, L_P + i * 1024 + lane * 16);
  __builtin_amdgcn_sched_barrier(0);
  pv_mma(a0, pf[0], pf[1], o);
  pv_load(L, 2, tb, a0);
  pv_mma(a1, pf[2], pf[3], o);
  pv_mma(a0, pf[4], pf[5], o);
}
DI bool lge3(LAS unsigned char* L, unsigned off, unsigned want) {
  const v4u f = *(volatile LAS v4u*)(L + off);
  return (unsigned)__builtin_amdgcn_readfirstlane(min(min(f.x, f.y), f.z)) >= want;
}

__global__ void __launch_bounds__(512, 2) mla_fused(Params p) {
  extern __shared__ __attribute__((aligned(16))) unsigned char smem[];
  LAS unsigned char* L = (LAS unsigned char*)smem;
#define PIN(id, us) do { } while (0)
  RT(0);
  const int tid = threadIdx.x, lane = tid & 63, w = __builtin_amdgcn_readfirstlane(tid >> 6);
  const int wg = blockIdx.x, xcd = wg & 7, idx = wg >> 3;
  const int br = xcd >> 2, b = xcd & 3, j = idx;
  const int h = (xcd & 3) * 8 + (idx >> 2), cs = idx & 3;
  const int rank = (xcd & 3) * 32 + idx;
  const int g = lane >> 4, n = lane & 15, hh = lane >> 5, r32 = lane & 31;
  const bf16_t* px = br ? p.x[1] : p.x[0]; const bf16_t* pckv = br ? p.ckv[1] : p.ckv[0]; const bf16_t* pwq = br ? p.wq[1] : p.wq[0];
  const bf16_t* pwuk = br ? p.wuk[1] : p.wuk[0]; const bf16_t* pwuv = br ? p.wuv[1] : p.wuv[0]; const bf16_t* pwo = br ? p.wo[1] : p.wo[0];
  unsigned char* wsb = p.ws + (size_t)br * WS_BR;
  bf16_t* pout = p.out;
  asm volatile("" :: "s"(px), "s"(pckv), "s"(pwq), "s"(pwuk), "s"(pwuv), "s"(pwo), "s"(wsb), "s"(pout));
  const __amdgpu_buffer_rsrc_t rs = mkrs(wsb, WS_BR);

  if (__builtin_expect(w < 4, 1)) {
    const int ww = w;
    __builtin_amdgcn_s_setprio(2);
    v4u xa[4], wqv[32], uk[8];
    const __amdgpu_buffer_rsrc_t rx = mkrs(px, 4096), rq = mkrs(pwq, 4u << 20), rk = mkrs(pwuk, 4u << 20);
    const unsigned vx = (n >> 2) * 1024 + ww * 64 + g * 16;
    const unsigned vq = (ww * 32 + g * 8) * 8192 + h * 256 + n * 16;
    const unsigned vk = (cs * 128 + ww * 32 + n) * 8192 + h * 256 + g * 16;
    const unsigned qo = WS_QA + ((n >> 2) * 32 + h) * 2048 + cs * 512 + ww * 128 + ((n & 1) * 4 + g) * 16;
#pragma unroll
    for (int i = 0; i < 8; ++i) wqv[i] = ldb(rq, vq, (((0 + cs) & 3) * 128 + i) * 8192);
    if ((n & 3) < 2) __builtin_amdgcn_raw_buffer_store_b128(v4u{0u, 0u, 0u, 0u}, rs, qo, 0, AUX_SC1);
    if (rank == 0) __builtin_amdgcn_raw_buffer_store_b128(v4u{0u, 0u, 0u, 0u}, rs, WS_C3 + hh * (64 * 256) + (ww * 16 + n) * 256 + (g & 1) * 16, 0, AUX_SC1);
#pragma unroll
    for (int q = 0; q < 4; ++q) xa[q] = ldb(rx, vx, ((q + cs) & 3) * 256);
#pragma unroll
    for (int i = 0; i < 4; ++i) uk[i] = ldb(rk, vk + i * 64, 0);
#pragma unroll
    for (int i = 0; i < 4; ++i) uk[4 + i] = ldb(rk, vk + i * 64, 16 * 8192);
#define BAR1(pos) do { if (BARPOS == (pos)) { __builtin_amdgcn_sched_barrier(0); __builtin_amdgcn_s_barrier(); __builtin_amdgcn_sched_barrier(0); } } while (0)
    __builtin_amdgcn_sched_barrier(0);
    BAR1(0);
    RT(1);
    u64 T;
    asm volatile("s_load_dwordx2 %0, %1, 0x0\n\ts_waitcnt lgkmcnt(0)" : "=s"(T) : "s"(wsb) : "memory");
    T += 2;
    f32x4 qa[8];
#pragma unroll
    for (int i = 0; i < 8; ++i) qa[i] = f32x4{0.f, 0.f, 0.f, 0.f};
    asm volatile("" : "+v"(wqv[7]));
    __builtin_amdgcn_sched_barrier(0);
    BAR1(1);
#pragma unroll
    for (int q = 1; q < 4; ++q)
#pragma unroll
      for (int i = 0; i < 8; ++i) wqv[8 * q + i] = ldb(rq, vq, (((q + cs) & 3) * 128 + i) * 8192);
    __builtin_amdgcn_sched_barrier(0);
    BAR1(2);
    RT(15);
    const unsigned Ta = (unsigned)T | 1u;
#pragma unroll
    for (int q = 0; q < 4; ++q)
#pragma unroll
      for (int i = 0; i < 8; ++i) qa[i] = mfma16(xa[q], tfrag(&wqv[8 * q], i), qa[i]);
    {
      v4u v; v.x = pk(qa[0][0], qa[1][0]); v.y = pk(qa[2][0], qa[3][0]); v.z = pk(qa[4][0], qa[5][0]); v.w = pk(qa[6][0], qa[7][0]);
      lds_st(L, L_QP + (4 * g + ww) * 272 + n * 16, v);
    }
    BAR1(3);
    lsig(L, F_QP + 4 * ww, 1u, lane);
    lwait4(L, F_QP, 1u);
    {
      v4u qb[4];
#pragma unroll
      for (int s = 0; s < 4; ++s) qb[s] = lds_ld(L, L_QP + n * 272 + s * 64 + g * 16);
      lsig(L, F_QC + 4 * ww, 1u, lane);
      f32x4 e0 = {0.f, 0.f, 0.f, 0.f}, e1 = {0.f, 0.f, 0.f, 0.f};
#pragma unroll
      for (int s = 0; s < 4; ++s) { e0 = mfma16(uk[s], qb[s], e0); e1 = mfma16(uk[4 + s], qb[s], e1); }
#pragma unroll
      for (int i = 0; i < 4; ++i) {
        e0[i] += dpp<0xB1>(e0[i]); e0[i] += dpp<0x4E>(e0[i]);
        e1[i] += dpp<0xB1>(e1[i]); e1[i] += dpp<0x4E>(e1[i]);
      }
      const int u = n & 3;
      const f32x4 e = (u & 1) ? e1 : e0;
      v4u o; o.x = pk(e[0] * QSCALE, e[1] * QSCALE); o.y = Ta; o.z = pk(e[2] * QSCALE, e[3] * QSCALE); o.w = Ta;
      vm0();
      if (u < 2) __builtin_amdgcn_raw_buffer_store_b128(o, rs, qo, 0, AUX_SC1);
    }
    RT(3);
    v4u qf[32];
    {
      v4u gq[16];
      const unsigned gv = WS_QA + (b * 32 + 8 * ww) * 2048 + lane * 16;
      __builtin_amdgcn_s_sleep(QFIRST);
      cbar();
      RT(2);
#pragma unroll
      for (int i = 0; i < 16; ++i) gq[i] = __builtin_amdgcn_raw_buffer_load_b128(rs, gv + (i & 3) * 1024, (i >> 2) * 4096, AUX_SC1);
      for (unsigned sp = 0; sp < SPIN_G; ++sp) {
        unsigned bad = 0;
#pragma unroll
        for (int i = 0; i < 16; ++i) bad |= (gq[i].y ^ Ta) | (gq[i].w ^ Ta);
        if (__all(bad == 0u)) break;
        __builtin_amdgcn_s_sleep(QSLEEP);
        cbar();
#pragma unroll
        for (int i = 0; i < 16; ++i) gq[i] = __builtin_amdgcn_raw_buffer_load_b128(rs, gv + (i & 3) * 1024, (i >> 2) * 4096, AUX_SC1);
      }
      cbar();
      RT(4);
      lwait4(L, F_QC, 1u);
      const unsigned sx = ((unsigned)(lane >> 2) & 3u) | (((unsigned)(lane >> 1) & 1u) << 2);
      const unsigned wb = L_STG + (unsigned)(lane >> 2) * 1024u + (((unsigned)lane >> 1) & 1u) * 512u + (lane & 1) * 8u + (unsigned)ww * 128u;
#pragma unroll
      for (int i = 0; i < 16; ++i) {
        v2u d; d.x = gq[i].x; d.y = gq[i].z;
        *(LAS v2u*)(L + wb + (i & 1) * 16384 + (((unsigned)(i >> 1) ^ sx) * 16u)) = d;
      }
    }
    lsig(L, F_QS + 4 * ww, 1u, lane);
    const unsigned fr = ((r32 & 3) << 2) | ((r32 >> 2) & 3);
    const unsigned rowb = 1024u * r32 + 16u * ((unsigned)hh ^ fr);
    unsigned ab[8], qb4[4];
#pragma unroll
    for (int s = 0; s < 8; ++s) { ab[s] = (L_C + 32768u * ww + rowb) ^ (32u * s); asm volatile("" : "+v"(ab[s])); }
#pragma unroll
    for (int s = 0; s < 4; ++s) { qb4[s] = L_STG + 512u * hh + (((unsigned)r32 ^ ((unsigned)s | ((unsigned)hh << 2))) * 16u); asm volatile("" : "+v"(qb4[s])); }
    lwait4(L, F_QS, 1u);
    PIN(1, 25);
    f32x16 sp;
#pragma unroll
    for (int i = 0; i < 16; ++i) sp[i] = 0.f;
    if (lge4(L, F_LAND, 8u * (ww + 1))) {
      cbar();
      RT(7);
      v4u af[32];
#pragma unroll
      for (int s = 0; s < 6; ++s) { qf[s] = lds_ld(L, qb4[s & 3] + 1024u * s); af[s] = lds_ld(L, ab[s]); }
      __builtin_amdgcn_sched_barrier(0);
#pragma unroll
      for (int s = 0; s < 32; ++s) {
        if (s + 6 < 32) { qf[s + 6] = lds_ld(L, qb4[(s + 6) & 3] + 1024u * (s + 6)); af[s + 6] = lds_ld(L, ab[(s + 6) & 7] + 256u * ((s + 6) >> 3)); }
        sp = mfma32(af[s], qf[s], sp);
        if (s == 0) lsig(L, F_Q0 + 4 * ww, 1u, lane);
        __builtin_amdgcn_sched_barrier(0);
      }
      lsig(L, F_QR + 4 * ww, 1u, lane);
    } else {
      qf[0] = lds_ld(L, qb4[0]);
      lsig(L, F_Q0 + 4 * ww, 1u, lane);
#pragma unroll
      for (int i = 1; i < 32; ++i) qf[i] = lds_ld(L, qb4[i & 3] + 1024u * i);
      lgkm0();
      lsig(L, F_QR + 4 * ww, 1u, lane);
      lwait4(L, F_LAND, 8u * (ww + 1));
      RT(7);
      v4u af[32];
#pragma unroll
      for (int s = 0; s < 8; ++s) af[s] = lds_ld(L, ab[s]);
      __builtin_amdgcn_sched_barrier(0);
#pragma unroll
      for (int s = 0; s < 32; ++s) {
        if (s + 8 < 32) af[s + 8] = lds_ld(L, ab[s & 7] + 256u * ((s + 8) >> 3));
        sp = mfma32(af[s], qf[s], sp);
        __builtin_amdgcn_sched_barrier(0);
      }
    }
    {
      float mx = fmaxf(fmaxf(sp[0], sp[1]), fmaxf(sp[2], sp[3]));
#pragma unroll
      for (int i = 4; i < 16; i += 2) mx = fmaxf(mx, fmaxf(sp[i], sp[i + 1]));
      float mrun = __builtin_ceilf(swap32_max(mx));
      { const unsigned mb = __float_as_uint(mrun); if (lane < 32) *(volatile LAS unsigned*)(L + L_MT + lane * 16 + ww * 4) = mb == SENT ? 0x7FC00000u : mb; }
      v4u mt;
      for (unsigned s = 0; s < SPIN_L; ++s) {
        mt = *(volatile LAS v4u*)(L + L_MT + r32 * 16);
        if (__all(max(max(mt.x, mt.y), mt.z) != SENT)) break;
      }
      mrun = fmaxf(fmaxf(mrun, __uint_as_float(mt.x)), fmaxf(__uint_as_float(mt.y), __uint_as_float(mt.z)));
      float pe[16];
#pragma unroll
      for (int i = 0; i < 16; ++i) pe[i] = __builtin_amdgcn_exp2f(sp[i] - mrun);
      v4u p0, p1;
      p0.x = pk(pe[0], pe[1]); p0.y = pk(pe[2], pe[3]); p0.z = pk(pe[4], pe[5]); p0.w = pk(pe[6], pe[7]);
      p1.x = pk(pe[8], pe[9]); p1.y = pk(pe[10], pe[11]); p1.z = pk(pe[12], pe[13]); p1.w = pk(pe[14], pe[15]);
      lwait4(L, F_QR, 1u);
      lds_st(L, L_P + ww * 2048 + lane * 16, p0); lds_st(L, L_P + ww * 2048 + 1024 + lane * 16, p1);
      lsig(L, F_P + 4 * ww, 1u, lane);
      float ls = (((pe[0] + pe[1]) + (pe[2] + pe[3])) + ((pe[4] + pe[5]) + (pe[6] + pe[7]))) + (((pe[8] + pe[9]) + (pe[10] + pe[11])) + ((pe[12] + pe[13]) + (pe[14] + pe[15])));
      ls = swap32_sum(ls);
      if (lane < 32) *(LAS float*)(L + L_LT + lane * 16 + ww * 4) = ls;
      lsig(L, F_P + 4 * ww, 2u, lane);
    }
    RT(8);
    PIN(3, 30);
    {
      v4u ov[8];
#pragma unroll
      for (int i = 0; i < 8; ++i) ov[i] = v4u{0u, 0u, 0u, 0u};
      const unsigned oo = ((ww * 32 + g * 8) * 4 + cs) * 8192 + h * 256 + n * 16;
      unsigned char* cp = wsb + WS_C3 + ((cs * 4 + ww) * 4 + (h & 3)) * 256 + (lane & 7) * 4;
      unsigned got = 0;
      v2u rc = {0u, 0u};
      const unsigned mo = ((ww * 32 + r32) * 4 + cs) * 256 + h * 8;
      for (unsigned sp = 0; sp < SPIN_G; ++sp) {
        unsigned r = 0; const unsigned z = 0;
        if (lane < 8) asm volatile("global_atomic_add %0, %1, %2, off sc0 sc1\n\ts_waitcnt vmcnt(0)" : "=&v"(r) : "v"(cp), "v"(z) : "memory");
        const unsigned nw = (unsigned)__builtin_amdgcn_ballot_w64(r >= 4u) & ~got;
#pragma unroll
        for (int i = 0; i < 8; ++i)
          if (nw & (1u << i)) asm volatile("global_load_dwordx4 %0, %1, %2 sc1" : "+v"(ov[i]) : "v"(oo), "s"(wsb + WS_OP + i * 32768) : "memory");
        if ((nw >> (lane & 7)) & 1u) asm volatile("global_load_dwordx2 %0, %1, %2 sc1" : "+v"(rc) : "v"(mo), "s"(wsb + WS_ML) : "memory");
        got |= nw;
        if (got == 0xFFu) break;
        __builtin_amdgcn_s_sleep(RSLEEP);
      }
      RT(11);
      asm volatile("s_waitcnt vmcnt(0)" : "+v"(rc), "+v"(ov[0]), "+v"(ov[1]), "+v"(ov[2]), "+v"(ov[3]), "+v"(ov[4]), "+v"(ov[5]), "+v"(ov[6]), "+v"(ov[7]) :: "memory");
      const float mj = __uint_as_float(rc.x), lj = __uint_as_float(rc.y);
      float M = mj;
      M = fmaxf(M, dpp<0xB1>(M)); M = fmaxf(M, dpp<0x4E>(M)); M = fmaxf(M, dpp<0x124>(M)); M = fmaxf(M, dpp<0x128>(M));
      M = swap16_max(M);
      const float ej = __builtin_amdgcn_exp2f(mj - M);
      float Ls = ej * lj;
      Ls += dpp<0xB1>(Ls); Ls += dpp<0x4E>(Ls); Ls += dpp<0x124>(Ls); Ls += dpp<0x128>(Ls);
      Ls = swap16_sum(Ls);
      const float inv = 1.0f / Ls;
      if (lane < 32) *(LAS unsigned short*)(L + L_WB + ww * 64 + lane * 2) = (unsigned short)(__float_as_uint(ej) >> 16);
      cbar();
      const v4u wf = *(volatile LAS v4u*)(L + L_WB + ww * 64 + g * 16);
      float c8[8];
#pragma unroll
      for (int i = 0; i < 8; ++i) { const f32x4 d = mfma16(wf, tfrag(ov, i), f32x4{0.f, 0.f, 0.f, 0.f}); c8[i] = d[0] * inv; }
      v4u v; v.x = pk(c8[0], c8[1]); v.y = pk(c8[2], c8[3]); v.z = pk(c8[4], c8[5]); v.w = pk(c8[6], c8[7]);
      if (lane < 16) lds_st(L, L_CC + ww * 272 + n * 16, v);
    }
    lsig(L, F_CC + 4 * ww, 1u, lane);
    RT(12);
    if (ww == 0) {
      const int u2 = h & 1, bo = (h >> 1) & 3, a4 = h >> 3;
      const unsigned so = WS_OUTP + (lane >> 2) * 8192 + cs * 2048 + u2 * 1024 + (bo * 16 + a4 * 4 + (lane & 3)) * 16;
      {
        unsigned char* cp = wsb + WS_C4 + ((cs * 4 + (lane & 3)) * 4 + (h & 3)) * 256;
        for (unsigned sp = 0; sp < SPIN_G; ++sp) {
          unsigned r = 32u; const unsigned z = 0;
          if (lane < 4) asm volatile("global_atomic_add %0, %1, %2, off sc0 sc1\n\ts_waitcnt vmcnt(0)" : "=&v"(r) : "v"(cp), "v"(z) : "memory");
          if (__all(r >= 32u)) break;
          __builtin_amdgcn_s_sleep(RSLEEP);
        }
        cbar();
      }
      RT(13);
      v4u pv[8];
      for (unsigned sp = 0; sp < SPIN_G; ++sp) {
        cbar();
#pragma unroll
        for (int i = 0; i < 8; ++i) pv[i] = __builtin_amdgcn_raw_buffer_load_b128(rs, so, i * (16 * 8192), AUX_SC1);
        unsigned m = 0;
#pragma unroll
        for (int i = 0; i < 8; ++i) m = max(m, umax4(pv[i]));
        if (__all(m != SENT)) break;
        __builtin_amdgcn_s_sleep(OSLEEP);
      }
      cbar();
      RT(5);
      f32x4 s = ((__builtin_bit_cast(f32x4, pv[0]) + __builtin_bit_cast(f32x4, pv[1])) + (__builtin_bit_cast(f32x4, pv[2]) + __builtin_bit_cast(f32x4, pv[3]))) +
                ((__builtin_bit_cast(f32x4, pv[4]) + __builtin_bit_cast(f32x4, pv[5])) + (__builtin_bit_cast(f32x4, pv[6]) + __builtin_bit_cast(f32x4, pv[7])));
#pragma unroll
      for (int i = 0; i < 4; ++i) {
        s[i] += dpp<0x124>(s[i]); s[i] += dpp<0x128>(s[i]);
        s[i] = swap32_sum(swap16_sum(s[i]));
      }
      if (lane < 4) {
        v2u o2; o2.x = pk(s[0], s[1]); o2.y = pk(s[2], s[3]);
        *(GAS v2u*)(pout + bo * 1024 + br * 512 + cs * 128 + (a4 * 4 + lane) * 8 + u2 * 4) = o2;
      }
      if (rank == 0 && lane == 0) __hip_atomic_store((GAS u64*)(wsb + WS_EPOCH), T, __ATOMIC_RELAXED, __HIP_MEMORY_SCOPE_AGENT);
      RT(6);
    }
  } else {
    const int aw = w - 4;
    const unsigned tk0 = (unsigned)__builtin_amdgcn_s_memrealtime();
    if (aw == 1) {
      if (lane < 48) *(volatile LAS unsigned*)(L + L_FL + lane * 4) = 0u;
      *(volatile LAS v2u*)(L + L_MT + lane * 8) = v2u{SENT, SENT};
    }
    lgkm0();
    RT(1);
    __builtin_amdgcn_s_barrier();
    RT(2);
    v4u r127 = {0u, 0u, 0u, 0u};
    {
      const bf16_t* C = pckv + ((size_t)b * 4096 + j * 128) * 512;
#define DMA_ROW(r4_, k_) glds16(C + (size_t)(16 * (r4_) + 4 * (k_) + aw) * 512 + (unsigned)(lane ^ (((unsigned)aw << 2) | (unsigned)(k_))) * 8, \
                                (unsigned)(size_t)smem + L_C + (16 * (r4_) + 4 * (k_) + aw) * 1024)
#pragma unroll
      for (int i = 0; i < 32; ++i) {
        if (i == 31 && aw == 3) r127 = *(const GAS v4u*)(C + (size_t)127 * 512 + (unsigned)(lane ^ 15) * 8);
        else DMA_ROW(i >> 2, i & 3);
        const int win = i < THRSW ? THR : THR2, pwin = (i - 1) < THRSW ? THR : THR2;
        if (win == THR) asm volatile("s_waitcnt vmcnt(%0)" :: "n"(THR) : "memory"); else asm volatile("s_waitcnt vmcnt(%0)" :: "n"(THR2) : "memory");
        int done = i + 1 - win, best = 0;
        for (int k = 0; k < i; ++k) { const int d = k + 1 - (k < THRSW ? THR : THR2); if (d > best) best = d; }
        (void)pwin;
        if (done > 0 && (done >> 3) > (best > 0 ? best >> 3 : 0)) { if (lane == 0) *(volatile LAS unsigned*)(L + F_LAND + 4 * aw) = (unsigned)(done & ~7); }
      }
      if (32 - THR2 < 24) {
        asm volatile("s_waitcnt vmcnt(8)" ::: "memory");
        if (lane == 0) *(volatile LAS unsigned*)(L + F_LAND + 4 * aw) = 24u;
      }
    }
    RT(3);
    unsigned tb[8];
    {
      const unsigned blk = (lane >> 4) & 1, q = (lane & 15) >> 2, pp = lane & 3;
      const unsigned b0 = L_C + 1024u * (4u * hh + q) + 256u * aw + 64u * q + 16u * ((2u * blk + (pp >> 1)) ^ (unsigned)hh) + 8u * (pp & 1);
#pragma unroll
      for (int tau = 0; tau < 4; ++tau) { tb[2 * tau] = b0 ^ (64u * tau); tb[2 * tau + 1] = b0 ^ (64u * tau) ^ 32u; }
    }
    f32x16 o[4];
#pragma unroll
    for (int tau = 0; tau < 4; ++tau)
#pragma unroll
      for (int i = 0; i < 16; ++i) o[tau][i] = 0.f;
    __builtin_amdgcn_s_setprio(APRIO);
    const bool early = lge3(L, F_P, 1u);
    if (early) { cbar(); s16x4 a0[16], a1[16]; pv_load(L, 0, tb, a0); pv_load(L, 1, tb, a1); pv012(L, tb, lane, a0, a1, o); }
    RT(4);
    vm0();
    if (aw == 3) { lwait4(L, F_Q0, 1u); lds_st(L, L_C + 127 * 1024 + lane * 16, r127); cbar(); }
    if (lane == 0) *(volatile LAS unsigned*)(L + F_LAND + 4 * aw) = 32u;
    RT(5);
    v4u uv[8], wov[32];
    const __amdgpu_buffer_rsrc_t rv = mkrs(pwuv, 4u << 20), ro = mkrs(pwo, 4u << 20);
    const unsigned vv = (cs * 128 + aw * 32 + g * 8) * 8192 + h * 256 + n * 16;
    const unsigned vo = (h * 128 + g * 8) * 1024 + aw * 256 + n * 16;
    const bool wnow = !early && (int)((unsigned)__builtin_amdgcn_s_memrealtime() - tk0) < WGATE;
    if (wnow) {
#pragma unroll
      for (int i = 0; i < 8; ++i) uv[i] = ldb(rv, vv, i * 8192);
#pragma unroll
      for (int i = 0; i < 8; ++i) wov[i] = ldb(ro, vo, (cs * 32 + i) * 1024);
    }
    __builtin_amdgcn_sched_barrier(0);
    if (!early) {
      s16x4 a0[16], a1[16];
      lwait4(L, F_LAND, 24u);
      pv_load(L, 0, tb, a0); pv_load(L, 1, tb, a1);
      for (unsigned s = 0; s < SPIN_L; ++s) if (lge3(L, F_P, 1u)) break;
      cbar();
      RT(4);
      pv012(L, tb, lane, a0, a1, o);
    }
    {
      s16x4 pa[16];
      lwait4(L, F_LAND, 32u);
      pv_load(L, 3, tb, pa);
      RT(6);
      v4u mt;
      for (unsigned s = 0; s < SPIN_L; ++s) {
        mt = *(volatile LAS v4u*)(L + L_MT + r32 * 16);
        if (__all(mt.w != SENT)) break;
      }
      const float m012 = fmaxf(fmaxf(__uint_as_float(mt.x), __uint_as_float(mt.y)), __uint_as_float(mt.z)), mf = fmaxf(m012, __uint_as_float(mt.w));
      const float al = __builtin_amdgcn_exp2f(m012 - mf);
#pragma unroll
      for (int tau = 0; tau < 4; ++tau) { o[tau] *= al; asm volatile("" : "+v"(o[tau])); }
      unsigned wa[4];
#pragma unroll
      for (int k = 0; k < 4; ++k) { wa[k] = L_C + 1024u * r32 + 256u * aw + 16u * (((unsigned)(2 * k) + (unsigned)hh) ^ (unsigned)(r32 & 7)); asm volatile("" : "+v"(wa[k])); }
      unsigned ra[2];
#pragma unroll
      for (int k = 0; k < 2; ++k) { ra[k] = L_C + 1024u * (4u * k + g) + 256u * aw + 16u * (unsigned)(n ^ (4 * k + g)); asm volatile("" : "+v"(ra[k])); }
      lwait1(L, F_P + 12, 1u);
      RT(7);
      const v4u p0 = lds_ld(L, L_P + 6144 + lane * 16), p1 = lds_ld(L, L_P + 7168 + lane * 16);
      __builtin_amdgcn_sched_barrier(0);
#pragma unroll
      for (int tau = 0; tau < 4; ++tau) {
        o[tau] = mfma32(__builtin_bit_cast(v4u, __builtin_shufflevector(pa[4 * tau], pa[4 * tau + 1], 0, 1, 2, 3, 4, 5, 6, 7)), p0, o[tau]);
        o[tau] = mfma32(__builtin_bit_cast(v4u, __builtin_shufflevector(pa[4 * tau + 2], pa[4 * tau + 3], 0, 1, 2, 3, 4, 5, 6, 7)), p1, o[tau]);
      }
#pragma unroll
      for (int tau = 0; tau < 4; ++tau)
#pragma unroll
        for (int v = 0; v < 2; ++v) {
          const unsigned ax = pk(o[tau][8 * v + 0], o[tau][8 * v + 1]), ay = pk(o[tau][8 * v + 2], o[tau][8 * v + 3]);
          const unsigned bx = pk(o[tau][8 * v + 4], o[tau][8 * v + 5]), by = pk(o[tau][8 * v + 6], o[tau][8 * v + 7]);
          auto sx = __builtin_amdgcn_permlane32_swap(ax, bx, false, false);
          auto sy = __builtin_amdgcn_permlane32_swap(ay, by, false, false);
          v4u val; val.x = sx[0]; val.y = sy[0]; val.z = sx[1]; val.w = sy[1];
          lds_st(L, wa[(2 * tau + v) & 3] + 128u * ((2 * tau + v) >> 2), val);
        }
      cbar();
      RT(8);
      v4u ov[8];
#pragma unroll
      for (int i = 0; i < 8; ++i) ov[i] = lds_ld(L, ra[i & 1] + 8192u * (i >> 1));
#pragma unroll
      for (int i = 0; i < 8; ++i) __builtin_amdgcn_raw_buffer_store_b128(ov[i], rs, WS_OP + ((b * 32 + j) * 4 + aw) * 8192 + lane * 16 + (i & 3) * 1024, (i >> 2) * 4096, AUX_SC1);
      lwait4(L, F_P, 2u);
      const f32x4 lt = *(LAS f32x4*)(L + L_LT + r32 * 16);
      v2u ml; ml.x = __float_as_uint(mf); ml.y = __float_as_uint(((lt[0] + lt[1]) + lt[2]) * al + lt[3]);
      if (lane < 32) __builtin_amdgcn_raw_buffer_store_b64(ml, rs, WS_ML + ((b * 32 + j) * 4 + aw) * 256 + lane * 8, 0, AUX_SC1);
    }
    RT(9);
    vm0();
    RT(10);
    if (lane < 4) __hip_atomic_fetch_add((GAS unsigned*)(wsb + WS_C3 + ((aw * 4 + b) * 4 + lane) * 256 + (j & 7) * 4), 1u, __ATOMIC_RELAXED, __HIP_MEMORY_SCOPE_AGENT);
    if (!wnow) {
#pragma unroll
      for (int i = 0; i < 8; ++i) uv[i] = ldb(rv, vv, i * 8192);
#pragma unroll
      for (int i = 0; i < 8; ++i) wov[i] = ldb(ro, vo, (cs * 32 + i) * 1024);
    }
    {
      const v4u sv = {SENT, SENT, SENT, SENT};
      __builtin_amdgcn_raw_buffer_store_b128(sv, rs, WS_OUTP + rank * 8192 + aw * 2048 + lane * 16, 0, AUX_SC1);
      __builtin_amdgcn_raw_buffer_store_b128(sv, rs, WS_OUTP + rank * 8192 + aw * 2048 + 1024 + lane * 16, 0, AUX_SC1);
    }
#pragma unroll
    for (int q = 1; q < 4; ++q)
#pragma unroll
      for (int i = 0; i < 8; ++i) wov[8 * q + i] = ldb(ro, vo, (((q + cs) & 3) * 32 + i) * 1024);
    __builtin_amdgcn_sched_barrier(0);
    v4u uvf[8], wf[32];
#pragma unroll
    for (int i = 0; i < 8; ++i) uvf[i] = tfrag(uv, i);
#pragma unroll
    for (int q = 0; q < 4; ++q)
#pragma unroll
      for (int i = 0; i < 8; ++i) wf[8 * q + i] = tfrag(&wov[8 * q], i);
#pragma unroll
    for (int i = 0; i < 8; ++i) asm volatile("" : "+v"(uvf[i]));
#pragma unroll
    for (int i = 0; i < 32; ++i) asm volatile("" : "+v"(wf[i]));
    vm0();
    RT(11);
    lwait4(L, F_CC, 1u);
    RT(12);
    {
      const v4u af = lds_ld(L, L_CC + (n >> 2) * 272 + aw * 64 + g * 16);
      float c8[8];
#pragma unroll
      for (int i = 0; i < 8; ++i) { const f32x4 d = mfma16(af, uvf[i], f32x4{0.f, 0.f, 0.f, 0.f}); c8[i] = d[0]; }
      v4u v; v.x = pk(c8[0], c8[1]); v.y = pk(c8[2], c8[3]); v.z = pk(c8[4], c8[5]); v.w = pk(c8[6], c8[7]);
      lds_st(L, L_CT + (4 * g + aw) * 272 + n * 16, v);
    }
#define C4ADD() do { if (lane < 4) __hip_atomic_fetch_add((GAS unsigned*)(wsb + WS_C4 + ((aw * 4 + (rank >> 5)) * 4 + lane) * 256), 1u, __ATOMIC_RELAXED, __HIP_MEMORY_SCOPE_AGENT); } while (0)
    lsig(L, F_CT + 4 * aw, 1u, lane);
    lwait4(L, F_CT, 1u);
    RT(14);
    {
      v4u af[4];
#pragma unroll
      for (int q = 0; q < 4; ++q) af[q] = lds_ld(L, L_CT + n * 272 + ((q + cs) & 3) * 64 + g * 16);
      C4ADD();
      f32x4 d[8];
#pragma unroll
      for (int i = 0; i < 8; ++i) d[i] = f32x4{0.f, 0.f, 0.f, 0.f};
#pragma unroll
      for (int q = 0; q < 4; ++q)
#pragma unroll
        for (int i = 0; i < 8; ++i) d[i] = mfma16(af[q], wf[8 * q + i], d[i]);
      v4u s0, s1;
      unsigned sv[8];
#pragma unroll
      for (int i = 0; i < 8; ++i) { const unsigned x = __float_as_uint((d[i][0] + d[i][1]) + (d[i][2] + d[i][3])); sv[i] = x == SENT ? 0x7FC00000u : x; }
      s0.x = sv[0]; s0.y = sv[1]; s0.z = sv[2]; s0.w = sv[3]; s1.x = sv[4]; s1.y = sv[5]; s1.z = sv[6]; s1.w = sv[7];
      __builtin_amdgcn_raw_buffer_store_b128(s0, rs, WS_OUTP + rank * 8192 + aw * 2048 + lane * 16, 0, AUX_SC1);
      __builtin_amdgcn_raw_buffer_store_b128(s1, rs, WS_OUTP + rank * 8192 + aw * 2048 + 1024 + lane * 16, 0, AUX_SC1);
    }
    RT(13);
  }
}
}

extern "C" void kernel_launch(void* const* d_in, const int* in_sizes, int n_in, void* d_out, int out_size, void* d_ws, size_t ws_size,
                              hipStream_t stream) {
  (void)in_sizes; (void)n_in; (void)out_size; (void)ws_size;
  mk::Params p;
  for (int br = 0; br < 2; ++br) {
    p.x[br] = (const bf16_t*)d_in[6 * br + 0]; p.ckv[br] = (const bf16_t*)d_in[6 * br + 1]; p.wq[br] = (const bf16_t*)d_in[6 * br + 2];
    p.wuk[br] = (const bf16_t*)d_in[6 * br + 3]; p.wuv[br] = (const bf16_t*)d_in[6 * br + 4]; p.wo[br] = (const bf16_t*)d_in[6 * br + 5];
  }
  p.ws = (unsigned char*)d_ws; p.out = (bf16_t*)d_out;
  (void)hipFuncSetAttribute((const void*)mk::mla_fused, hipFuncAttributeMaxDynamicSharedMemorySize, (int)mk::LDS_BYTES);
  mk::mla_fused<<<256, 512, mk::LDS_BYTES, stream>>>(p);
}
```

```cpp
#include <hip/hip_runtime.h>
#include <stdint.h>

#define GAS __attribute__((address_space(1)))
#define LAS __attribute__((address_space(3)))
#define DI __device__ __forceinline__
typedef unsigned short bf16_t;
typedef unsigned v4u __attribute__((ext_vector_type(4)));
typedef unsigned v2u __attribute__((ext_vector_type(2)));
typedef float f32x2 __attribute__((ext_vector_type(2)));
typedef float f32x4 __attribute__((ext_vector_type(4)));
typedef float f32x16 __attribute__((ext_vector_type(16)));
typedef short bf16x8 __attribute__((ext_vector_type(8)));
typedef short s16x4 __attribute__((ext_vector_type(4)));
typedef __bf16 bf16x2_t __attribute__((ext_vector_type(2)));
typedef unsigned long long u64;

namespace mk {

constexpr unsigned WS_EPOCH = 0;
constexpr unsigned WS_C3 = 4096;
constexpr unsigned WS_C4 = WS_C3 + 64 * 256;
constexpr unsigned WS_ML = WS_C4 + 64 * 256;
constexpr unsigned WS_QA = WS_ML + 4 * 32 * 4 * 256;
constexpr unsigned WS_OUTP = WS_QA + 4 * 32 * 2048;
constexpr unsigned WS_OP = WS_OUTP + 128 * 8192;
constexpr unsigned WS_BR = 8u << 20;
static_assert(WS_OP + 32 * 4 * 4 * 32 * 256 <= WS_BR, "ws map");
constexpr unsigned WS_TOTAL = 2 * WS_BR;

constexpr unsigned L_C = 0;
constexpr unsigned L_STG = 127 * 1024;
constexpr unsigned L_P = 131072;
constexpr unsigned L_CC = L_P + 8192;
constexpr unsigned L_CT = L_CC + 4 * 272;
constexpr unsigned L_QP = L_CT;
constexpr unsigned L_FL = 159 * 1024;
constexpr unsigned F_LAND = L_FL + 0;
constexpr unsigned F_QP = L_FL + 16;
constexpr unsigned F_QC = L_FL + 32;
constexpr unsigned F_QS = L_FL + 48;
constexpr unsigned F_QR = L_FL + 64;
constexpr unsigned F_Q0 = L_FL + 80;
constexpr unsigned F_P = L_FL + 96;
constexpr unsigned F_CC = L_FL + 128;
constexpr unsigned F_CT = L_FL + 144;
constexpr unsigned L_MT = L_FL + 512;
constexpr unsigned L_WB = L_CT + 16 * 272;
constexpr unsigned L_LT = L_WB + 256;
constexpr unsigned LDS_BYTES = 163840;
static_assert(L_LT + 512 <= L_FL && L_MT + 512 <= LDS_BYTES && (L_MT % 16) == 0 && (L_CT % 16) == 0, "lds map");

constexpr int AUX_SC1 = 16;
constexpr unsigned SENT = 0xFFFFFFFFu;
constexpr float QSCALE = 0.08838834764831845f * 1.4426950408889634f;
constexpr unsigned SPIN_G = 1u << 16, SPIN_L = 1u << 21;
constexpr int THR = 8, THR2 = 16, THRSW = 12;
constexpr int QFIRST = 50, QSLEEP = 8, RSLEEP = 2, OSLEEP = 4;

struct Params {
  const bf16_t* x[2]; const bf16_t* ckv[2]; const bf16_t* wq[2]; const bf16_t* wuk[2]; const bf16_t* wuv[2]; const bf16_t* wo[2];
  unsigned char* ws; bf16_t* out;
};
static_assert(sizeof(Params) == 112, "no padding");


DI __amdgpu_buffer_rsrc_t mkrs(const void* p, unsigned bytes) { return __builtin_amdgcn_make_buffer_rsrc((void*)p, 0, (int)bytes, 0x00020000); }
DI v4u ldb(__amdgpu_buffer_rsrc_t r, unsigned voff, unsigned soff) { return __builtin_amdgcn_raw_buffer_load_b128(r, (int)voff, (int)soff, 0); }
DI f32x4 mfma16(v4u a, v4u b, f32x4 c) { return __builtin_amdgcn_mfma_f32_16x16x32_bf16(__builtin_bit_cast(bf16x8, a), __builtin_bit_cast(bf16x8, b), c, 0, 0, 0); }
DI f32x16 mfma32(v4u a, v4u b, f32x16 c) { return __builtin_amdgcn_mfma_f32_32x32x16_bf16(__builtin_bit_cast(bf16x8, a), __builtin_bit_cast(bf16x8, b), c, 0, 0, 0); }
DI unsigned pk(float lo, float hi) { f32x2 v = {lo, hi}; return __builtin_bit_cast(unsigned, __builtin_convertvector(v, bf16x2_t)); }
DI void cbar() { asm volatile("" ::: "memory"); }
DI void lgkm0() { asm volatile("s_waitcnt lgkmcnt(0)" ::: "memory"); }
DI void vm0() { asm volatile("s_waitcnt vmcnt(0)" ::: "memory"); }
DI v4u tfrag(const v4u* R, int i) {
  const unsigned sel = (i & 1) ? 0x07060302u : 0x05040100u; const int u = i >> 1;
  v4u f;
  f.x = __builtin_amdgcn_perm(R[1][u], R[0][u], sel); f.y = __builtin_amdgcn_perm(R[3][u], R[2][u], sel);
  f.z = __builtin_amdgcn_perm(R[5][u], R[4][u], sel); f.w = __builtin_amdgcn_perm(R[7][u], R[6][u], sel);
  return f;
}
DI void glds16(const void* gsrc, unsigned lds_dst) {
  unsigned keep;
  asm volatile("s_mov_b32 %0, m0\n\ts_mov_b32 m0, %2\n\ts_nop 0\n\tglobal_load_lds_dwordx4 %1, off\n\ts_mov_b32 m0, %0" : "=&s"(keep) : "v"(gsrc), "s"(lds_dst) : "memory");
}
DI v4u lds_ld(LAS unsigned char* L, unsigned off) { return *(LAS v4u*)(L + off); }
DI void lds_st(LAS unsigned char* L, unsigned off, v4u v) { *(LAS v4u*)(L + off) = v; }
DI void lsig(LAS unsigned char* L, unsigned off, unsigned val, int lane) {
  cbar();
  if (lane == 0) *(volatile LAS unsigned*)(L + off) = val;
  cbar();
}
DI bool lge4(LAS unsigned char* L, unsigned off, unsigned want) {
  const v4u f = *(volatile LAS v4u*)(L + off);
  const unsigned m = min(min(f.x, f.y), min(f.z, f.w));
  return (unsigned)__builtin_amdgcn_readfirstlane(m) >= want;
}
DI void lwait4(LAS unsigned char* L, unsigned off, unsigned want) {
  for (unsigned s = 0; s < SPIN_L; ++s) if (lge4(L, off, want)) break;
  cbar();
}
DI void lwait1(LAS unsigned char* L, unsigned off, unsigned want) {
  for (unsigned s = 0; s < SPIN_L; ++s) if ((unsigned)__builtin_amdgcn_readfirstlane(*(volatile LAS unsigned*)(L + off)) >= want) break;
  cbar();
}
template <int CTRL> DI float dpp(float x) { return __builtin_bit_cast(float, __builtin_amdgcn_mov_dpp(__builtin_bit_cast(int, x), CTRL, 0xf, 0xf, true)); }
DI float swap32_max(float x) { auto t = __builtin_amdgcn_permlane32_swap(__float_as_uint(x), __float_as_uint(x), false, false); return fmaxf(__uint_as_float(t[0]), __uint_as_float(t[1])); }
DI float swap16_max(float x) { auto t = __builtin_amdgcn_permlane16_swap(__float_as_uint(x), __float_as_uint(x), false, false); return fmaxf(__uint_as_float(t[0]), __uint_as_float(t[1])); }
DI float swap32_sum(float x) { auto t = __builtin_amdgcn_permlane32_swap(__float_as_uint(x), __float_as_uint(x), false, false); return __uint_as_float(t[0]) + __uint_as_float(t[1]); }
DI float swap16_sum(float x) { auto t = __builtin_amdgcn_permlane16_swap(__float_as_uint(x), __float_as_uint(x), false, false); return __uint_as_float(t[0]) + __uint_as_float(t[1]); }
DI unsigned umax4(v4u v) { return max(max(v.x, v.y), max(v.z, v.w)); }

DI void pv_load(LAS unsigned char* L, int t, const unsigned (&tb)[8], s16x4 (&a)[16]) {
  const unsigned toff = (unsigned)t * 32768u;
#pragma unroll
  for (int tau = 0; tau < 4; ++tau)
#pragma unroll
    for (int s = 0; s < 2; ++s)
#pragma unroll
      for (int u = 0; u < 2; ++u)
        a[4 * tau + 2 * s + u] = __builtin_amdgcn_ds_read_tr16_b64_v4i16((LAS s16x4*)(L + (tb[2 * tau + u] + toff) + (8192u * u + 16384u * s)));
  __builtin_amdgcn_sched_barrier(0);
}
DI void pv_mma(const s16x4 (&a)[16], v4u p0, v4u p1, f32x16 (&o)[4]) {
#pragma unroll
  for (int tau = 0; tau < 4; ++tau) {
    o[tau] = mfma32(__builtin_bit_cast(v4u, __builtin_shufflevector(a[4 * tau], a[4 * tau + 1], 0, 1, 2, 3, 4, 5, 6, 7)), p0, o[tau]);
    o[tau] = mfma32(__builtin_bit_cast(v4u, __builtin_shufflevector(a[4 * tau + 2], a[4 * tau + 3], 0, 1, 2, 3, 4, 5, 6, 7)), p1, o[tau]);
  }
  __builtin_amdgcn_sched_barrier(0);
}
DI void pv012(LAS unsigned char* L, const unsigned (&tb)[8], int lane, s16x4 (&a0)[16], s16x4 (&a1)[16], f32x16 (&o)[4]) {
  v4u pf[6];
#pragma unroll
  for (int i = 0; i < 6; ++i) pf[i] = lds_ld(L, L_P + i * 1024 + lane * 16);
  __builtin_amdgcn_sched_barrier(0);
  pv_mma(a0, pf[0], pf[1], o);
  pv_load(L, 2, tb, a0);
  pv_mma(a1, pf[2], pf[3], o);
  pv_mma(a0, pf[4], pf[5], o);
}
DI bool lge3(LAS unsigned char* L, unsigned off, unsigned want) {
  const v4u f = *(volatile LAS v4u*)(L + off);
  return (unsigned)__builtin_amdgcn_readfirstlane(min(min(f.x, f.y), f.z)) >= want;
}

__global__ void __launch_bounds__(512, 2) mla_fused(Params p) {
  extern __shared__ __attribute__((aligned(16))) unsigned char smem[];
  LAS unsigned char* L = (LAS unsigned char*)smem;
  const int tid = threadIdx.x, lane = tid & 63, w = __builtin_amdgcn_readfirstlane(tid >> 6);
  const int wg = blockIdx.x, xcd = wg & 7, idx = wg >> 3;
  const int br = xcd >> 2, b = xcd & 3, j = idx;
  const int h = (xcd & 3) * 8 + (idx >> 2), cs = idx & 3;
  const int rank = (xcd & 3) * 32 + idx;
  const int g = lane >> 4, n = lane & 15, hh = lane >> 5, r32 = lane & 31;
  const bf16_t* px = br ? p.x[1] : p.x[0]; const bf16_t* pckv = br ? p.ckv[1] : p.ckv[0]; const bf16_t* pwq = br ? p.wq[1] : p.wq[0];
  const bf16_t* pwuk = br ? p.wuk[1] : p.wuk[0]; const bf16_t* pwuv = br ? p.wuv[1] : p.wuv[0]; const bf16_t* pwo = br ? p.wo[1] : p.wo[0];
  unsigned char* wsb = p.ws + (size_t)br * WS_BR;
  bf16_t* pout = p.out;
  asm volatile("" :: "s"(px), "s"(pckv), "s"(pwq), "s"(pwuk), "s"(pwuv), "s"(pwo), "s"(wsb), "s"(pout));
  const __amdgpu_buffer_rsrc_t rs = mkrs(wsb, WS_BR);

  if (__builtin_expect(w < 4, 1)) {
    const int ww = w;
    __builtin_amdgcn_s_setprio(2);
    v4u xa[4], wqv[32], uk[8];
    const __amdgpu_buffer_rsrc_t rx = mkrs(px, 4096), rq = mkrs(pwq, 4u << 20), rk = mkrs(pwuk, 4u << 20);
    const unsigned vx = (n >> 2) * 1024 + ww * 64 + g * 16;
    const unsigned vq = (ww * 32 + g * 8) * 8192 + h * 256 + n * 16;
    const unsigned vk = (cs * 128 + ww * 32 + n) * 8192 + h * 256 + g * 16;
    const unsigned qo = WS_QA + ((n >> 2) * 32 + h) * 2048 + cs * 512 + ww * 128 + ((n & 1) * 4 + g) * 16;
#pragma unroll
    for (int i = 0; i < 8; ++i) wqv[i] = ldb(rq, vq, (((0 + cs) & 3) * 128 + i) * 8192);
    if ((n & 3) < 2) __builtin_amdgcn_raw_buffer_store_b128(v4u{0u, 0u, 0u, 0u}, rs, qo, 0, AUX_SC1);
    if (rank == 0) __builtin_amdgcn_raw_buffer_store_b128(v4u{0u, 0u, 0u, 0u}, rs, WS_C3 + hh * (64 * 256) + (ww * 16 + n) * 256 + (g & 1) * 16, 0, AUX_SC1);
#pragma unroll
    for (int q = 0; q < 4; ++q) xa[q] = ldb(rx, vx, ((q + cs) & 3) * 256);
#pragma unroll
    for (int i = 0; i < 4; ++i) uk[i] = ldb(rk, vk + i * 64, 0);
#pragma unroll
    for (int i = 0; i < 4; ++i) uk[4 + i] = ldb(rk, vk + i * 64, 16 * 8192);
    __builtin_amdgcn_sched_barrier(0);
    __builtin_amdgcn_s_barrier();
    __builtin_amdgcn_sched_barrier(0);
    u64 T;
    asm volatile("s_load_dwordx2 %0, %1, 0x0\n\ts_waitcnt lgkmcnt(0)" : "=s"(T) : "s"(wsb) : "memory");
    T += 2;
    f32x4 qa[8];
#pragma unroll
    for (int i = 0; i < 8; ++i) qa[i] = f32x4{0.f, 0.f, 0.f, 0.f};
    asm volatile("" : "+v"(wqv[7]));
    __builtin_amdgcn_sched_barrier(0);
#pragma unroll
    for (int q = 1; q < 4; ++q)
#pragma unroll
      for (int i = 0; i < 8; ++i) wqv[8 * q + i] = ldb(rq, vq, (((q + cs) & 3) * 128 + i) * 8192);
    __builtin_amdgcn_sched_barrier(0);
    const unsigned Ta = (unsigned)T | 1u;
#pragma unroll
    for (int q = 0; q < 4; ++q)
#pragma unroll
      for (int i = 0; i < 8; ++i) qa[i] = mfma16(xa[q], tfrag(&wqv[8 * q], i), qa[i]);
    {
      v4u v; v.x = pk(qa[0][0], qa[1][0]); v.y = pk(qa[2][0], qa[3][0]); v.z = pk(qa[4][0], qa[5][0]); v.w = pk(qa[6][0], qa[7][0]);
      lds_st(L, L_QP + (4 * g + ww) * 272 + n * 16, v);
    }
    lsig(L, F_QP + 4 * ww, 1u, lane);
    lwait4(L, F_QP, 1u);
    {
      v4u qb[4];
#pragma unroll
      for (int s = 0; s < 4; ++s) qb[s] = lds_ld(L, L_QP + n * 272 + s * 64 + g * 16);
      lsig(L, F_QC + 4 * ww, 1u, lane);
      f32x4 e0 = {0.f, 0.f, 0.f, 0.f}, e1 = {0.f, 0.f, 0.f, 0.f};
#pragma unroll
      for (int s = 0; s < 4; ++s) { e0 = mfma16(uk[s], qb[s], e0); e1 = mfma16(uk[4 + s], qb[s], e1); }
#pragma unroll
      for (int i = 0; i < 4; ++i) {
        e0[i] += dpp<0xB1>(e0[i]); e0[i] += dpp<0x4E>(e0[i]);
        e1[i] += dpp<0xB1>(e1[i]); e1[i] += dpp<0x4E>(e1[i]);
      }
      const int u = n & 3;
      const f32x4 e = (u & 1) ? e1 : e0;
      v4u o; o.x = pk(e[0] * QSCALE, e[1] * QSCALE); o.y = Ta; o.z = pk(e[2] * QSCALE, e[3] * QSCALE); o.w = Ta;
      vm0();
      if (u < 2) __builtin_amdgcn_raw_buffer_store_b128(o, rs, qo, 0, AUX_SC1);
    }
    v4u qf[32];
    {
      v4u gq[16];
      const unsigned gv = WS_QA + (b * 32 + 8 * ww) * 2048 + lane * 16;
      __builtin_amdgcn_s_sleep(QFIRST);
      cbar();
#pragma unroll
      for (int i = 0; i < 16; ++i) gq[i] = __builtin_amdgcn_raw_buffer_load_b128(rs, gv + (i & 3) * 1024, (i >> 2) * 4096, AUX_SC1);
      for (unsigned sp = 0; sp < SPIN_G; ++sp) {
        unsigned bad = 0;
#pragma unroll
        for (int i = 0; i < 16; ++i) bad |= (gq[i].y ^ Ta) | (gq[i].w ^ Ta);
        if (__all(bad == 0u)) break;
        __builtin_amdgcn_s_sleep(QSLEEP);
        cbar();
#pragma unroll
        for (int i = 0; i < 16; ++i) gq[i] = __builtin_amdgcn_raw_buffer_load_b128(rs, gv + (i & 3) * 1024, (i >> 2) * 4096, AUX_SC1);
      }
      cbar();
      lwait4(L, F_QC, 1u);
      const unsigned sx = ((unsigned)(lane >> 2) & 3u) | (((unsigned)(lane >> 1) & 1u) << 2);
      const unsigned wb = L_STG + (unsigned)(lane >> 2) * 1024u + (((unsigned)lane >> 1) & 1u) * 512u + (lane & 1) * 8u + (unsigned)ww * 128u;
#pragma unroll
      for (int i = 0; i < 16; ++i) {
        v2u d; d.x = gq[i].x; d.y = gq[i].z;
        *(LAS v2u*)(L + wb + (i & 1) * 16384 + (((unsigned)(i >> 1) ^ sx) * 16u)) = d;
      }
    }
    lsig(L, F_QS + 4 * ww, 1u, lane);
    const unsigned fr = ((r32 & 3) << 2) | ((r32 >> 2) & 3);
    const unsigned rowb = 1024u * r32 + 16u * ((unsigned)hh ^ fr);
    unsigned ab[8], qb4[4];
#pragma unroll
    for (int s = 0; s < 8; ++s) { ab[s] = (L_C + 32768u * ww + rowb) ^ (32u * s); asm volatile("" : "+v"(ab[s])); }
#pragma unroll
    for (int s = 0; s < 4; ++s) { qb4[s] = L_STG + 512u * hh + (((unsigned)r32 ^ ((unsigned)s | ((unsigned)hh << 2))) * 16u); asm volatile("" : "+v"(qb4[s])); }
    lwait4(L, F_QS, 1u);
    f32x16 sp;
#pragma unroll
    for (int i = 0; i < 16; ++i) sp[i] = 0.f;
    if (lge4(L, F_LAND, 8u * (ww + 1))) {
      cbar();
      v4u af[32];
#pragma unroll
      for (int s = 0; s < 6; ++s) { qf[s] = lds_ld(L, qb4[s & 3] + 1024u * s); af[s] = lds_ld(L, ab[s]); }
      __builtin_amdgcn_sched_barrier(0);
#pragma unroll
      for (int s = 0; s < 32; ++s) {
        if (s + 6 < 32) { qf[s + 6] = lds_ld(L, qb4[(s + 6) & 3] + 1024u * (s + 6)); af[s + 6] = lds_ld(L, ab[(s + 6) & 7] + 256u * ((s + 6) >> 3)); }
        sp = mfma32(af[s], qf[s], sp);
        if (s == 0) lsig(L, F_Q0 + 4 * ww, 1u, lane);
        __builtin_amdgcn_sched_barrier(0);
      }
      lsig(L, F_QR + 4 * ww, 1u, lane);
    } else {
      qf[0] = lds_ld(L, qb4[0]);
      lsig(L, F_Q0 + 4 * ww, 1u, lane);
#pragma unroll
      for (int i = 1; i < 32; ++i) qf[i] = lds_ld(L, qb4[i & 3] + 1024u * i);
      lgkm0();
      lsig(L, F_QR + 4 * ww, 1u, lane);
      lwait4(L, F_LAND, 8u * (ww + 1));
      v4u af[32];
#pragma unroll
      for (int s = 0; s < 8; ++s) af[s] = lds_ld(L, ab[s]);
      __builtin_amdgcn_sched_barrier(0);
#pragma unroll
      for (int s = 0; s < 32; ++s) {
        if (s + 8 < 32) af[s + 8] = lds_ld(L, ab[s & 7] + 256u * ((s + 8) >> 3));
        sp = mfma32(af[s], qf[s], sp);
        __builtin_amdgcn_sched_barrier(0);
      }
    }
    {
      float mx = fmaxf(fmaxf(sp[0], sp[1]), fmaxf(sp[2], sp[3]));
#pragma unroll
      for (int i = 4; i < 16; i += 2) mx = fmaxf(mx, fmaxf(sp[i], sp[i + 1]));
      float mrun = __builtin_ceilf(swap32_max(mx));
      { const unsigned mb = __float_as_uint(mrun); if (lane < 32) *(volatile LAS unsigned*)(L + L_MT + lane * 16 + ww * 4) = mb == SENT ? 0x7FC00000u : mb; }
      v4u mt;
      for (unsigned s = 0; s < SPIN_L; ++s) {
        mt = *(volatile LAS v4u*)(L + L_MT + r32 * 16);
        if (__all(max(max(mt.x, mt.y), mt.z) != SENT)) break;
      }
      mrun = fmaxf(fmaxf(mrun, __uint_as_float(mt.x)), fmaxf(__uint_as_float(mt.y), __uint_as_float(mt.z)));
      float pe[16];
#pragma unroll
      for (int i = 0; i < 16; ++i) pe[i] = __builtin_amdgcn_exp2f(sp[i] - mrun);
      v4u p0, p1;
      p0.x = pk(pe[0], pe[1]); p0.y = pk(pe[2], pe[3]); p0.z = pk(pe[4], pe[5]); p0.w = pk(pe[6], pe[7]);
      p1.x = pk(pe[8], pe[9]); p1.y = pk(pe[10], pe[11]); p1.z = pk(pe[12], pe[13]); p1.w = pk(pe[14], pe[15]);
      lwait4(L, F_QR, 1u);
      lds_st(L, L_P + ww * 2048 + lane * 16, p0); lds_st(L, L_P + ww * 2048 + 1024 + lane * 16, p1);
      lsig(L, F_P + 4 * ww, 1u, lane);
      float ls = (((pe[0] + pe[1]) + (pe[2] + pe[3])) + ((pe[4] + pe[5]) + (pe[6] + pe[7]))) + (((pe[8] + pe[9]) + (pe[10] + pe[11])) + ((pe[12] + pe[13]) + (pe[14] + pe[15])));
      ls = swap32_sum(ls);
      if (lane < 32) *(LAS float*)(L + L_LT + lane * 16 + ww * 4) = ls;
      lsig(L, F_P + 4 * ww, 2u, lane);
    }
    {
      v4u ov[8];
#pragma unroll
      for (int i = 0; i < 8; ++i) ov[i] = v4u{0u, 0u, 0u, 0u};
      const unsigned oo = ((ww * 32 + g * 8) * 4 + cs) * 8192 + h * 256 + n * 16;
      unsigned char* cp = wsb + WS_C3 + ((cs * 4 + ww) * 4 + (h & 3)) * 256 + (lane & 7) * 4;
      unsigned got = 0;
      v2u rc = {0u, 0u};
      const unsigned mo = ((ww * 32 + r32) * 4 + cs) * 256 + h * 8;
      for (unsigned sp = 0; sp < SPIN_G; ++sp) {
        unsigned r = 0; const unsigned z = 0;
        if (lane < 8) asm volatile("global_atomic_add %0, %1, %2, off sc0 sc1\n\ts_waitcnt vmcnt(0)" : "=&v"(r) : "v"(cp), "v"(z) : "memory");
        const unsigned nw = (unsigned)__builtin_amdgcn_ballot_w64(r >= 4u) & ~got;
#pragma unroll
        for (int i = 0; i < 8; ++i)
          if (nw & (1u << i)) asm volatile("global_load_dwordx4 %0, %1, %2 sc1" : "+v"(ov[i]) : "v"(oo), "s"(wsb + WS_OP + i * 32768) : "memory");
        if ((nw >> (lane & 7)) & 1u) asm volatile("global_load_dwordx2 %0, %1, %2 sc1" : "+v"(rc) : "v"(mo), "s"(wsb + WS_ML) : "memory");
        got |= nw;
        if (got == 0xFFu) break;
        __builtin_amdgcn_s_sleep(RSLEEP);
      }
      asm volatile("s_waitcnt vmcnt(0)" : "+v"(rc), "+v"(ov[0]), "+v"(ov[1]), "+v"(ov[2]), "+v"(ov[3]), "+v"(ov[4]), "+v"(ov[5]), "+v"(ov[6]), "+v"(ov[7]) :: "memory");
      const float mj = __uint_as_float(rc.x), lj = __uint_as_float(rc.y);
      float M = mj;
      M = fmaxf(M, dpp<0xB1>(M)); M = fmaxf(M, dpp<0x4E>(M)); M = fmaxf(M, dpp<0x124>(M)); M = fmaxf(M, dpp<0x128>(M));
      M = swap16_max(M);
      const float ej = __builtin_amdgcn_exp2f(mj - M);
      float Ls = ej * lj;
      Ls += dpp<0xB1>(Ls); Ls += dpp<0x4E>(Ls); Ls += dpp<0x124>(Ls); Ls += dpp<0x128>(Ls);
      Ls = swap16_sum(Ls);
      const float inv = 1.0f / Ls;
      if (lane < 32) *(LAS unsigned short*)(L + L_WB + ww * 64 + lane * 2) = (unsigned short)(__float_as_uint(ej) >> 16);
      cbar();
      const v4u wf = *(volatile LAS v4u*)(L + L_WB + ww * 64 + g * 16);
      float c8[8];
#pragma unroll
      for (int i = 0; i < 8; ++i) { const f32x4 d = mfma16(wf, tfrag(ov, i), f32x4{0.f, 0.f, 0.f, 0.f}); c8[i] = d[0] * inv; }
      v4u v; v.x = pk(c8[0], c8[1]); v.y = pk(c8[2], c8[3]); v.z = pk(c8[4], c8[5]); v.w = pk(c8[6], c8[7]);
      if (lane < 16) lds_st(L, L_CC + ww * 272 + n * 16, v);
    }
    lsig(L, F_CC + 4 * ww, 1u, lane);
    if (ww == 0) {
      const int u2 = h & 1, bo = (h >> 1) & 3, a4 = h >> 3;
      const unsigned so = WS_OUTP + (lane >> 2) * 8192 + cs * 2048 + u2 * 1024 + (bo * 16 + a4 * 4 + (lane & 3)) * 16;
      {
        unsigned char* cp = wsb + WS_C4 + ((cs * 4 + (lane & 3)) * 4 + (h & 3)) * 256;
        for (unsigned sp = 0; sp < SPIN_G; ++sp) {
          unsigned r = 32u; const unsigned z = 0;
          if (lane < 4) asm volatile("global_atomic_add %0, %1, %2, off sc0 sc1\n\ts_waitcnt vmcnt(0)" : "=&v"(r) : "v"(cp), "v"(z) : "memory");
          if (__all(r >= 32u)) break;
          __builtin_amdgcn_s_sleep(RSLEEP);
        }
        cbar();
      }
      v4u pv[8];
      for (unsigned sp = 0; sp < SPIN_G; ++sp) {
        cbar();
#pragma unroll
        for (int i = 0; i < 8; ++i) pv[i] = __builtin_amdgcn_raw_buffer_load_b128(rs, so, i * (16 * 8192), AUX_SC1);
        unsigned m = 0;
#pragma unroll
        for (int i = 0; i < 8; ++i) m = max(m, umax4(pv[i]));
        if (__all(m != SENT)) break;
        __builtin_amdgcn_s_sleep(OSLEEP);
      }
      cbar();
      f32x4 s = ((__builtin_bit_cast(f32x4, pv[0]) + __builtin_bit_cast(f32x4, pv[1])) + (__builtin_bit_cast(f32x4, pv[2]) + __builtin_bit_cast(f32x4, pv[3]))) +
                ((__builtin_bit_cast(f32x4, pv[4]) + __builtin_bit_cast(f32x4, pv[5])) + (__builtin_bit_cast(f32x4, pv[6]) + __builtin_bit_cast(f32x4, pv[7])));
#pragma unroll
      for (int i = 0; i < 4; ++i) {
        s[i] += dpp<0x124>(s[i]); s[i] += dpp<0x128>(s[i]);
        s[i] = swap32_sum(swap16_sum(s[i]));
      }
      if (lane < 4) {
        v2u o2; o2.x = pk(s[0], s[1]); o2.y = pk(s[2], s[3]);
        *(GAS v2u*)(pout + bo * 1024 + br * 512 + cs * 128 + (a4 * 4 + lane) * 8 + u2 * 4) = o2;
      }
      if (rank == 0 && lane == 0) __hip_atomic_store((GAS u64*)(wsb + WS_EPOCH), T, __ATOMIC_RELAXED, __HIP_MEMORY_SCOPE_AGENT);
    }
  } else {
    const int aw = w - 4;
    if (aw == 1) {
      if (lane < 48) *(volatile LAS unsigned*)(L + L_FL + lane * 4) = 0u;
      *(volatile LAS v2u*)(L + L_MT + lane * 8) = v2u{SENT, SENT};
    }
    lgkm0();
    __builtin_amdgcn_s_barrier();
    v4u r127 = {0u, 0u, 0u, 0u};
    {
      const bf16_t* C = pckv + ((size_t)b * 4096 + j * 128) * 512;
#define DMA_ROW(r4_, k_) glds16(C + (size_t)(16 * (r4_) + 4 * (k_) + aw) * 512 + (unsigned)(lane ^ (((unsigned)aw << 2) | (unsigned)(k_))) * 8, \
                                (unsigned)(size_t)smem + L_C + (16 * (r4_) + 4 * (k_) + aw) * 1024)
#pragma unroll
      for (int i = 0; i < 32; ++i) {
        if (i == 31 && aw == 3) r127 = *(const GAS v4u*)(C + (size_t)127 * 512 + (unsigned)(lane ^ 15) * 8);
        else DMA_ROW(i >> 2, i & 3);
        const int win = i < THRSW ? THR : THR2;
        if (win == THR) asm volatile("s_waitcnt vmcnt(%0)" :: "n"(THR) : "memory"); else asm volatile("s_waitcnt vmcnt(%0)" :: "n"(THR2) : "memory");
        int done = i + 1 - win, best = 0;
        for (int k = 0; k < i; ++k) { const int d = k + 1 - (k < THRSW ? THR : THR2); if (d > best) best = d; }
        if (done > 0 && (done >> 3) > (best > 0 ? best >> 3 : 0)) { if (lane == 0) *(volatile LAS unsigned*)(L + F_LAND + 4 * aw) = (unsigned)(done & ~7); }
      }
      if (32 - THR2 < 16) {
        asm volatile("s_waitcnt vmcnt(16)" ::: "memory");
        if (lane == 0) *(volatile LAS unsigned*)(L + F_LAND + 4 * aw) = 16u;
      }
      if (32 - THR2 < 24) {
        asm volatile("s_waitcnt vmcnt(8)" ::: "memory");
        if (lane == 0) *(volatile LAS unsigned*)(L + F_LAND + 4 * aw) = 24u;
      }
    }
    unsigned tb[8];
    {
      const unsigned blk = (lane >> 4) & 1, q = (lane & 15) >> 2, pp = lane & 3;
      const unsigned b0 = L_C + 1024u * (4u * hh + q) + 256u * aw + 64u * q + 16u * ((2u * blk + (pp >> 1)) ^ (unsigned)hh) + 8u * (pp & 1);
#pragma unroll
      for (int tau = 0; tau < 4; ++tau) { tb[2 * tau] = b0 ^ (64u * tau); tb[2 * tau + 1] = b0 ^ (64u * tau) ^ 32u; }
    }
    f32x16 o[4];
#pragma unroll
    for (int tau = 0; tau < 4; ++tau)
#pragma unroll
      for (int i = 0; i < 16; ++i) o[tau][i] = 0.f;
    __builtin_amdgcn_s_setprio(1);
    const bool early = lge3(L, F_P, 1u);
    if (early) { cbar(); s16x4 a0[16], a1[16]; pv_load(L, 0, tb, a0); pv_load(L, 1, tb, a1); pv012(L, tb, lane, a0, a1, o); }
    vm0();
    if (aw == 3) { lwait4(L, F_Q0, 1u); lds_st(L, L_C + 127 * 1024 + lane * 16, r127); cbar(); }
    if (lane == 0) *(volatile LAS unsigned*)(L + F_LAND + 4 * aw) = 32u;
    v4u uv[8], wov[32];
    const __amdgpu_buffer_rsrc_t rv = mkrs(pwuv, 4u << 20), ro = mkrs(pwo, 4u << 20);
    const unsigned vv = (cs * 128 + aw * 32 + g * 8) * 8192 + h * 256 + n * 16;
    const unsigned vo = (h * 128 + g * 8) * 1024 + aw * 256 + n * 16;
    const bool wnow = !early;
    if (wnow) {
#pragma unroll
      for (int i = 0; i < 8; ++i) uv[i] = ldb(rv, vv, i * 8192);
#pragma unroll
      for (int i = 0; i < 8; ++i) wov[i] = ldb(ro, vo, (cs * 32 + i) * 1024);
    }
    __builtin_amdgcn_sched_barrier(0);
    if (!early) {
      s16x4 a0[16], a1[16];
      lwait4(L, F_LAND, 24u);
      pv_load(L, 0, tb, a0); pv_load(L, 1, tb, a1);
      for (unsigned s = 0; s < SPIN_L; ++s) if (lge3(L, F_P, 1u)) break;
      cbar();
      pv012(L, tb, lane, a0, a1, o);
    }
    {
      s16x4 pa[16];
      lwait4(L, F_LAND, 32u);
      pv_load(L, 3, tb, pa);
      v4u mt;
      for (unsigned s = 0; s < SPIN_L; ++s) {
        mt = *(volatile LAS v4u*)(L + L_MT + r32 * 16);
        if (__all(mt.w != SENT)) break;
      }
      const float m012 = fmaxf(fmaxf(__uint_as_float(mt.x), __uint_as_float(mt.y)), __uint_as_float(mt.z)), mf = fmaxf(m012, __uint_as_float(mt.w));
      const float al = __builtin_amdgcn_exp2f(m012 - mf);
#pragma unroll
      for (int tau = 0; tau < 4; ++tau) { o[tau] *= al; asm volatile("" : "+v"(o[tau])); }
      unsigned wa[4];
#pragma unroll
      for (int k = 0; k < 4; ++k) { wa[k] = L_C + 1024u * r32 + 256u * aw + 16u * (((unsigned)(2 * k) + (unsigned)hh) ^ (unsigned)(r32 & 7)); asm volatile("" : "+v"(wa[k])); }
      unsigned ra[2];
#pragma unroll
      for (int k = 0; k < 2; ++k) { ra[k] = L_C + 1024u * (4u * k + g) + 256u * aw + 16u * (unsigned)(n ^ (4 * k + g)); asm volatile("" : "+v"(ra[k])); }
      lwait1(L, F_P + 12, 1u);
      const v4u p0 = lds_ld(L, L_P + 6144 + lane * 16), p1 = lds_ld(L, L_P + 7168 + lane * 16);
      __builtin_amdgcn_sched_barrier(0);
#pragma unroll
      for (int tau = 0; tau < 4; ++tau) {
        o[tau] = mfma32(__builtin_bit_cast(v4u, __builtin_shufflevector(pa[4 * tau], pa[4 * tau + 1], 0, 1, 2, 3, 4, 5, 6, 7)), p0, o[tau]);
        o[tau] = mfma32(__builtin_bit_cast(v4u, __builtin_shufflevector(pa[4 * tau + 2], pa[4 * tau + 3], 0, 1, 2, 3, 4, 5, 6, 7)), p1, o[tau]);
      }
#pragma unroll
      for (int tau = 0; tau < 4; ++tau)
#pragma unroll
        for (int v = 0; v < 2; ++v) {
          const unsigned ax = pk(o[tau][8 * v + 0], o[tau][8 * v + 1]), ay = pk(o[tau][8 * v + 2], o[tau][8 * v + 3]);
          const unsigned bx = pk(o[tau][8 * v + 4], o[tau][8 * v + 5]), by = pk(o[tau][8 * v + 6], o[tau][8 * v + 7]);
          auto sx = __builtin_amdgcn_permlane32_swap(ax, bx, false, false);
          auto sy = __builtin_amdgcn_permlane32_swap(ay, by, false, false);
          v4u val; val.x = sx[0]; val.y = sy[0]; val.z = sx[1]; val.w = sy[1];
          lds_st(L, wa[(2 * tau + v) & 3] + 128u * ((2 * tau + v) >> 2), val);
        }
      cbar();
      v4u ov[8];
#pragma unroll
      for (int i = 0; i < 8; ++i) ov[i] = lds_ld(L, ra[i & 1] + 8192u * (i >> 1));
#pragma unroll
      for (int i = 0; i < 8; ++i) __builtin_amdgcn_raw_buffer_store_b128(ov[i], rs, WS_OP + ((b * 32 + j) * 4 + aw) * 8192 + lane * 16 + (i & 3) * 1024, (i >> 2) * 4096, AUX_SC1);
      lwait4(L, F_P, 2u);
      const f32x4 lt = *(LAS f32x4*)(L + L_LT + r32 * 16);
      v2u ml; ml.x = __float_as_uint(mf); ml.y = __float_as_uint(((lt[0] + lt[1]) + lt[2]) * al + lt[3]);
      if (lane < 32) __builtin_amdgcn_raw_buffer_store_b64(ml, rs, WS_ML + ((b * 32 + j) * 4 + aw) * 256 + lane * 8, 0, AUX_SC1);
    }
    vm0();
    if (lane < 4) __hip_atomic_fetch_add((GAS unsigned*)(wsb + WS_C3 + ((aw * 4 + b) * 4 + lane) * 256 + (j & 7) * 4), 1u, __ATOMIC_RELAXED, __HIP_MEMORY_SCOPE_AGENT);
    if (!wnow) {
#pragma unroll
      for (int i = 0; i < 8; ++i) uv[i] = ldb(rv, vv, i * 8192);
#pragma unroll
      for (int i = 0; i < 8; ++i) wov[i] = ldb(ro, vo, (cs * 32 + i) * 1024);
    }
    {
      const v4u sv = {SENT, SENT, SENT, SENT};
      __builtin_amdgcn_raw_buffer_store_b128(sv, rs, WS_OUTP + rank * 8192 + aw * 2048 + lane * 16, 0, AUX_SC1);
      __builtin_amdgcn_raw_buffer_store_b128(sv, rs, WS_OUTP + rank * 8192 + aw * 2048 + 1024 + lane * 16, 0, AUX_SC1);
    }
#pragma unroll
    for (int q = 1; q < 4; ++q)
#pragma unroll
      for (int i = 0; i < 8; ++i) wov[8 * q + i] = ldb(ro, vo, (((q + cs) & 3) * 32 + i) * 1024);
    __builtin_amdgcn_sched_barrier(0);
    v4u uvf[8], wf[32];
#pragma unroll
    for (int i = 0; i < 8; ++i) uvf[i] = tfrag(uv, i);
#pragma unroll
    for (int q = 0; q < 4; ++q)
#pragma unroll
      for (int i = 0; i < 8; ++i) wf[8 * q + i] = tfrag(&wov[8 * q], i);
#pragma unroll
    for (int i = 0; i < 8; ++i) asm volatile("" : "+v"(uvf[i]));
#pragma unroll
    for (int i = 0; i < 32; ++i) asm volatile("" : "+v"(wf[i]));
    vm0();
    lwait4(L, F_CC, 1u);
    {
      const v4u af = lds_ld(L, L_CC + (n >> 2) * 272 + aw * 64 + g * 16);
      float c8[8];
#pragma unroll
      for (int i = 0; i < 8; ++i) { const f32x4 d = mfma16(af, uvf[i], f32x4{0.f, 0.f, 0.f, 0.f}); c8[i] = d[0]; }
      v4u v; v.x = pk(c8[0], c8[1]); v.y = pk(c8[2], c8[3]); v.z = pk(c8[4], c8[5]); v.w = pk(c8[6], c8[7]);
      lds_st(L, L_CT + (4 * g + aw) * 272 + n * 16, v);
    }
#define C4ADD() do { if (lane < 4) __hip_atomic_fetch_add((GAS unsigned*)(wsb + WS_C4 + ((aw * 4 + (rank >> 5)) * 4 + lane) * 256), 1u, __ATOMIC_RELAXED, __HIP_MEMORY_SCOPE_AGENT); } while (0)
    lsig(L, F_CT + 4 * aw, 1u, lane);
    lwait4(L, F_CT, 1u);
    {
      v4u af[4];
#pragma unroll
      for (int q = 0; q < 4; ++q) af[q] = lds_ld(L, L_CT + n * 272 + ((q + cs) & 3) * 64 + g * 16);
      C4ADD();
      f32x4 d[8];
#pragma unroll
      for (int i = 0; i < 8; ++i) d[i] = f32x4{0.f, 0.f, 0.f, 0.f};
#pragma unroll
      for (int q = 0; q < 4; ++q)
#pragma unroll
        for (int i = 0; i < 8; ++i) d[i] = mfma16(af[q], wf[8 * q + i], d[i]);
      v4u s0, s1;
      unsigned sv[8];
#pragma unroll
      for (int i = 0; i < 8; ++i) { const unsigned x = __float_as_uint((d[i][0] + d[i][1]) + (d[i][2] + d[i][3])); sv[i] = x == SENT ? 0x7FC00000u : x; }
      s0.x = sv[0]; s0.y = sv[1]; s0.z = sv[2]; s0.w = sv[3]; s1.x = sv[4]; s1.y = sv[5]; s1.z = sv[6]; s1.w = sv[7];
      __builtin_amdgcn_raw_buffer_store_b128(s0, rs, WS_OUTP + rank * 8192 + aw * 2048 + lane * 16, 0, AUX_SC1);
      __builtin_amdgcn_raw_buffer_store_b128(s1, rs, WS_OUTP + rank * 8192 + aw * 2048 + 1024 + lane * 16, 0, AUX_SC1);
    }
  }
}
}

extern "C" void kernel_launch(void* const* d_in, const int* in_sizes, int n_in, void* d_out, int out_size, void* d_ws, size_t ws_size,
                              hipStream_t stream) {
  (void)in_sizes; (void)n_in; (void)out_size; (void)ws_size;
  mk::Params p;
  for (int br = 0; br < 2; ++br) {
    p.x[br] = (const bf16_t*)d_in[6 * br + 0]; p.ckv[br] = (const bf16_t*)d_in[6 * br + 1]; p.wq[br] = (const bf16_t*)d_in[6 * br + 2];
    p.wuk[br] = (const bf16_t*)d_in[6 * br + 3]; p.wuv[br] = (const bf16_t*)d_in[6 * br + 4]; p.wo[br] = (const bf16_t*)d_in[6 * br + 5];
  }
  p.ws = (unsigned char*)d_ws; p.out = (bf16_t*)d_out;
  (void)hipFuncSetAttribute((const void*)mk::mla_fused, hipFuncAttributeMaxDynamicSharedMemorySize, (int)mk::LDS_BYTES);
  mk::mla_fused<<<256, 512, mk::LDS_BYTES, stream>>>(p);
}
```
